```python
import torch

def _softmax(x, axis=-1):
    return torch.softmax(x, dim=axis)

def _gelu(x):
    return torch.nn.functional.gelu(x, approximate='tanh')

def _silu(x):
    return torch.nn.functional.silu(x)

def _sigmoid(x):
    return torch.sigmoid(x)

def _layernorm(x, axis=-1, eps=1e-05):
    m = x.mean(dim=axis, keepdim=True)
    v = x.var(dim=axis, keepdim=True, unbiased=False)
    return (x - m) / torch.sqrt(v + eps)

def _scatter_add(tgt, idx, src, axis=0):
    return tgt.clone().index_add_(axis, idx, src)

def _segment_sum(src, seg, n):
    out = torch.zeros((n,) + src.shape[1:], dtype=src.dtype)
    return out.index_add_(0, seg, src)

def _pad(x, pw):
    flat = tuple((v for pair in reversed(pw) for v in pair))
    return torch.nn.functional.pad(x, flat)
kernel_name = 'standalone__molecular_angular_painn__graph'

def setup_inputs():

    _w = lambda a: (lambda t: t.to(torch.bfloat16) if t.is_floating_point() else t)(torch.as_tensor(a))
    N, deg, D = (12500, 32, 128)
    n_rbf = max(16, 4)
    E = N * deg
    cutoff = 5.0
    freqs = (torch.arange(1, n_rbf + 1) * torch.pi / cutoff).to(float)
    return {'sv': _w((lambda *s: torch.randn(*s, dtype=torch.bfloat16) * 0.4)(N, 4, D) * 0.3), 'pos': _w((lambda *s: torch.randn(*s, dtype=torch.bfloat16) * 1.6)(N, 3) * 1.2), 'src': _w((lambda lo, hi, *s: torch.randint(lo, hi, s))(0, N, E).to(torch.int64)), 'dst': _w((lambda lo, hi, *s: torch.randint(lo, hi, s))(0, N, E).to(torch.int64)), 'freqs': _w(freqs), 'w_phi1': _w((lambda *s: torch.randn(*s, dtype=torch.bfloat16) * 3.2)(D, D) / D ** 0.5), 'w_phi2': _w((lambda *s: torch.randn(*s, dtype=torch.bfloat16) * 1.6)(D, 3 * D) / D ** 0.5), 'w_rbf': _w((lambda *s: torch.randn(*s, dtype=torch.bfloat16) * 1.6)(n_rbf, 3 * D) / n_rbf ** 0.5)}

def reference(sv, pos, src, dst, freqs, w_phi1, w_phi2, w_rbf):
    N, _, D = sv.shape
    E = src.shape[0]
    PI, cutoff = (3.141592653589793, 5.0)
    s, v = (sv[:, 0, :].clone(), sv[:, 1:, :].clone())
    r_vec = pos[src] - pos[dst]
    d = torch.sqrt((r_vec * r_vec).sum(-1) + 1e-08)
    r_hat = r_vec / d[:, None]
    rbf = torch.sin(d[:, None] * freqs[None, :]) / d[:, None]
    fcut = 0.5 * (torch.cos(PI * d / cutoff) + 1.0)
    fcut = torch.where(d < cutoff, fcut, 0.0 * d)
    Wf = rbf @ w_rbf * fcut[:, None]
    phi = (_silu(s @ w_phi1) @ w_phi2)[src]
    m = phi * Wf
    m_s, m_vv, m_vr = (m[:, :D].clone(), m[:, D:2 * D].clone(), m[:, 2 * D:].clone())
    ds = _segment_sum(m_s, dst, N)
    dv_e = m_vv[:, None, :] * v[src] + m_vr[:, None, :] * r_hat[:, :, None]
    dv = _segment_sum(dv_e.reshape(E, 3 * D), dst, N).reshape(N, 3, D)
    return sv + torch.concatenate([ds[:, None, :], dv], axis=1)
```

```cpp
#include <hip/hip_runtime.h>
#include <stdint.h>

typedef unsigned short u16;
typedef unsigned int u32;
typedef __attribute__((ext_vector_type(2))) u32 u32x2;
typedef __attribute__((ext_vector_type(4))) u32 u32x4;
typedef __attribute__((ext_vector_type(2))) float f32x2;
typedef __attribute__((ext_vector_type(4))) float f32x4;
typedef __attribute__((ext_vector_type(2))) __bf16 bf16x2;
#define LAS __attribute__((address_space(3)))
#pragma clang diagnostic ignored "-Wint-to-pointer-cast"

constexpr int BIN = 196, NBINS = 64, QN = 49;
constexpr int NPROD = 60, PCAP = 6720;
constexpr int RCAP = 112, RSTR = 1408;
constexpr int W_OFFS = 65536, W_RECS = 131072, W_OVF = W_RECS + NPROD * NBINS * RSTR, W_END = W_OVF + NPROD * PCAP * 16;
constexpr int LUT_BASE_BITS = 0x38D0;
constexpr int LUT_BYTES = 64000;
constexpr int COLDP = 29;
constexpr int NPRE = 4, DPOS = 4, NE = 16, NPB = 2, PFM = 1, PFE = 4, PFC = 5;
constexpr int MAXCH = 226;
constexpr int L_CNT = 0;
constexpr int L_HIST = 1024;
constexpr int L_MISC = 1280;
constexpr int L_SORT = 1344;
constexpr int L_QN = L_SORT + 1024;
constexpr int L_ITV = L_QN + 1024;
constexpr int L_ITS = L_ITV + 352;
constexpr int L_DUM = L_ITS + 256;
constexpr int L_OVL = L_DUM + 256;
constexpr int L_LUT = 6656;
constexpr int MAXOVL = (L_LUT - L_OVL) / 16;
constexpr int L_WS = L_LUT + LUT_BYTES, L_WV = L_WS + 4096;
constexpr int L_SRC = L_WV + 2048;
constexpr int L_REC = L_SRC + MAXCH * 128;
constexpr int L_END = L_REC + MAXCH * 256;
static_assert(L_QN - L_SRC == -74432 && L_REC == 105728 && L_REC % 256 == 0 && L_END <= 163840, "");

__device__ __forceinline__ float b2f(u32 x) { return __uint_as_float(x << 16); }
__device__ __forceinline__ float h2f(u32 x) { return __uint_as_float(x & 0xffff0000u); }
__device__ __forceinline__ u32 pk2(float lo, float hi) {
  f32x2 v = {lo, hi};
  bf16x2 r = __builtin_convertvector(v, bf16x2);
  return __builtin_bit_cast(u32, r);
}

typedef __attribute__((ext_vector_type(4))) short s16x4;
typedef __attribute__((ext_vector_type(8))) __bf16 bf16x8;
__device__ __forceinline__ u32 lds32(u32 a) { return *(LAS u32*)a; }
__device__ __forceinline__ u32x4 lds128(u32 a) { return *(LAS u32x4*)a; }
__device__ __forceinline__ void st32(u32 a, u32 v) { *(LAS u32*)a = v; }
__device__ __forceinline__ u32 ldsadd(u32 a, u32 v) { return __hip_atomic_fetch_add((LAS u32*)a, v, __ATOMIC_RELAXED, __HIP_MEMORY_SCOPE_WORKGROUP); }
__device__ __forceinline__ u32x4 mksrd(const void* p, u32 stride, u32 nrec, u32 w3) {
  uint64_t a = (uint64_t)p;
  u32x4 r = {(u32)a, ((u32)(a >> 32) & 0xffffu) | (stride << 16), nrec, w3};
  return r;
}
__device__ __forceinline__ int lane_id() { int l = __builtin_amdgcn_mbcnt_hi(~0u, __builtin_amdgcn_mbcnt_lo(~0u, 0u)); asm volatile("" : "+v"(l)); return l; }

#define BAR() do { asm volatile("s_waitcnt lgkmcnt(0)\n\ts_barrier" ::: "memory"); } while (0)
__device__ __forceinline__ float rbh(float f) { return __uint_as_float(pk2(0.f, f)); }
struct Geo { u32 r0, r1, r2, b4; float d; };
__device__ __forceinline__ u32x2 ldpos(u32 n) { u32 a = 512 + (__umul24(n, 6u) & ~3u); return u32x2{lds32(a), lds32(a + 4)}; }
__device__ __forceinline__ Geo geom(u32 s, u32 d, u32x2 ps, u32x2 pd) {
  u32 hs = s << 4, hd = d << 4;
  u32 sxy = __builtin_amdgcn_alignbit(ps[1], ps[0], hs), dxy = __builtin_amdgcn_alignbit(pd[1], pd[0], hd);
  float sz = b2f(ps[1] >> (hs & 31)), dz = b2f(pd[1] >> (hd & 31));
  float rx = rbh(b2f(sxy) - b2f(dxy)), ry = rbh(h2f(sxy) - h2f(dxy)), rz = rbh(sz - dz);
  float qq = rbh(rbh(rbh(rx * rx) + rbh(ry * ry) + rbh(rz * rz)) + 1e-8f);
  Geo o;
  o.d = rbh(__builtin_amdgcn_sqrtf(qq));
  float inv = __builtin_amdgcn_rcpf(o.d);
  o.b4 = (__umul24(d, 21400u) >> 20) & ~3u;
  o.r0 = pk2(rx * inv, ry * inv);
  o.r1 = pk2(rz * inv, o.d);
  o.r2 = (s << 8) + (d - __umul24(o.b4, 49u));
  return o;
}
__device__ __forceinline__ u32x4 ldstr2(u32 a, u32 d) {
  u32x2 x = __builtin_bit_cast(u32x2, __builtin_amdgcn_ds_read_tr16_b64_v4i16((LAS s16x4*)a));
  u32x2 y = __builtin_bit_cast(u32x2, __builtin_amdgcn_ds_read_tr16_b64_v4i16((LAS s16x4*)(a + d)));
  return u32x4{x[0], x[1], y[0], y[1]};
}
__device__ __forceinline__ f32x4 mfma(u32x4 a, u32x4 b, f32x4 c) {
  return __builtin_amdgcn_mfma_f32_16x16x32_bf16(__builtin_bit_cast(bf16x8, a), __builtin_bit_cast(bf16x8, b), c, 0, 0, 0);
}
__device__ __forceinline__ void dma16(u32x4 srd, u32 voff, u32 ldsbase) {
  u32 keep;
  asm volatile("s_mov_b32 %0, m0\n\ts_mov_b32 m0, %3\n\ts_nop 0\n\tbuffer_load_dwordx4 %1, %2, 0 offen lds\n\ts_mov_b32 m0, %0"
               : "=&s"(keep) : "v"(voff), "s"(srd), "s"(ldsbase) : "memory");
}
__device__ __forceinline__ u32 dith(float x, u32 lo) { return __builtin_elementwise_sub_sat(__float_as_uint(x) + 0x8000u, lo); }
__device__ __forceinline__ u32x4 dpack(float al0, float ah0, float al1, float ah1, float bl0, float bh0, float bl1, float bh1) {
  u32 l = pk2(al0, bl0);
  u32 a1 = dith(ah0, l & 0xffffu), a2 = dith(al1, a1 >> 16), a3 = dith(ah1, a2 >> 16);
  u32 b1 = dith(bh0, l >> 16), b2 = dith(bl1, b1 >> 16), b3 = dith(bh1, b2 >> 16);
  return u32x4{__builtin_amdgcn_perm(a1, l, 0x07060100u), __builtin_amdgcn_perm(a3, a2, 0x07060302u),
               __builtin_amdgcn_perm(b1, l, 0x07060302u), __builtin_amdgcn_perm(b3, b2, 0x07060302u)};
}

constexpr int M_W1 = 0, M_S = 32768, M_H = 49152, M_W2S = 65536, M_W2V = 98304;
constexpr int B_HIST = 0, B_POS = 512, B_R4 = B_POS + 75776, B_R8 = B_R4 + NBINS * RCAP * 4;
static_assert(B_R8 + NBINS * RCAP * 8 + 128 <= 163840 && B_R4 - B_R8 / 2 == 23808, "");
#define OPQ(x) asm("" : "+v"(x))
#define SBAR() do { asm volatile("s_barrier" ::: "memory"); } while (0)

__device__ __forceinline__ const u16* lut_addr(const u16* __restrict__ freqs, const u16* __restrict__ wrbf, int mb, int t) {
  int i = min(mb * 64 + (t & 63), 6143);
  int e = i & 7, lane = (i >> 3) & 63, blk = i >> 9, p = lane & 15, g = lane >> 4;
  int col = blk < 8 ? 128 * (1 + (blk & 1)) + 32 * (blk >> 1) + 2 * p : 8 * p + 2 * (blk - 8);
  return t >= 192 ? wrbf + ((8 * (g & 1) + e) * 384 + col + (g >> 1)) : freqs + (t & 15);
}
__device__ __forceinline__ float sinq(float x, int j) {
  if (__builtin_expect(!(fabsf(x) < 8192.0f), 0)) return j ? cosf(x) : sinf(x);
  float n = __builtin_rintf(x * 0.63661977236758134f);
  float r = fmaf(n, -1.5703125f, x);
  r = fmaf(n, -4.837512969970703125e-4f, r);
  r = fmaf(n, -7.54978995489188216e-8f, r);
  float z = r * r;
  float sn = fmaf(fmaf(fmaf(-1.9515295891e-4f, z, 8.3321608736e-3f), z, -1.6666654611e-1f) * z, r, r);
  float cs = fmaf(fmaf(fmaf(2.443315711809948e-5f, z, -1.388731625493765e-3f), z, 4.166664568298827e-2f) * z, z, fmaf(-0.5f, z, 1.0f));
  int q = (int)n + j;
  float y = (q & 1) ? cs : sn;
  return (q & 2) ? -y : y;
}

extern "C" __global__ __launch_bounds__(1024) void k_prep(const u16* sv, const u16* pos, const int* src, const int* dst,
                                               const u16* freqs, const u16* w1, const u16* w2, const u16* wrbf,
                                               char* ws, int N, int E, int EP) {
  const int t = threadIdx.x, lane = t & 63, w = __builtin_amdgcn_readfirstlane(t >> 6);
  const int p = lane & 15, g = lane >> 4;
  u32 cpf = 0;
  auto codepf = [&]() {
    u32 o = ((blockIdx.x >> 3) + (lane & 7) * 32) * 128;
    asm volatile("s_cmp_lg_u32 %2, %3\n\ts_cbranch_scc1 Lcp%=\n\ts_getpc_b64 s[86:87]\n\ts_add_u32 s86, s86, g_phi@rel32@lo+4\n\t"
                 "s_getpc_b64 s[84:85]\n\ts_add_u32 s84, s84, k_prep@rel32@lo+4\n\ts_addc_u32 s85, s85, k_prep@rel32@hi+12\n\t"
                 "s_sub_u32 s86, s86, s84\n\ts_cmp_lt_u32 s86, 0x10000\n\ts_cselect_b32 s86, s86, 0\n\t"
                 "v_min_u32 %1, s86, %1\n\t"
                 "global_load_dword %0, %1, s[84:85]\nLcp%=:"
                 : "+v"(cpf), "+v"(o) : "s"(w), "i"(15) : "s84", "s85", "s86", "s87", "scc");
  };
  codepf();
#define ARGS(tag) asm volatile("; " tag :: "s"(sv), "s"(pos), "s"(src), "s"(dst), "s"(freqs), "s"(w1), "s"(w2), "s"(wrbf), "s"(ws), "s"(N), "s"(E), "s"(EP)); \
  int N5 = N; asm volatile("; " tag : "+s"(N5)); char* tab = ws + LUT_BYTES; char* wfrag = ws + (LUT_BYTES + (size_t)N5 * 1280)
  if (__builtin_expect(blockIdx.x >= NPROD, 1)) {
    ARGS("mlp");
    const int nb = (blockIdx.x - NPROD) * 64, nv = __builtin_amdgcn_readfirstlane(max(0, min(64, N - nb)));
    const u32x4 srdS = mksrd((const char*)sv + (size_t)nb * 1024, 0, nv * 1024, 0x00020000u);
    const u32x4 srdW1 = mksrd(w1, 0, 32768, 0x00020000u), srdW2 = mksrd(w2, 0, 98304, 0x00020000u);
    const int mb = blockIdx.x - NPROD;
    const int u = w & 3, nt = w >> 2;
    u32x4 lq = {0, 0, 0, 0};
    const int q = p >> 2, pp = p & 3;
    const u32 sw = (q | ((g & 1) << 2));
    if (w >= 8) {
      const int w8 = w - 8;
      u32 warm = 0;
      if (w == 8) {
        asm volatile("global_load_dword %0, %1, %2" : "+v"(warm) : "v"(((((mb >> 3) * 32 + lane) % 768) * 128)), "s"(w2));
      }
      __builtin_amdgcn_s_sleep(8);
      u32x4 wv[8];
#pragma unroll
      for (int i = 0; i < 8; i++) {
        asm volatile("buffer_load_dwordx4 %0, %1, %2, %3 offen offset:256" : "=v"(wv[i]) : "v"((16 * w8 + (lane >> 5)) * 768 + (lane & 31) * 16), "s"(srdW2), "s"(i * 1536));
        if (i == NPRE - 1) SBAR();
      }
      asm volatile("" : "+v"(warm) :: "memory");
      __builtin_amdgcn_s_setprio(1);
      const u32 c = lane & 31, hi = lane >> 5;
#pragma unroll
      for (int i = 0; i < 8; i++) {
        if (i == DPOS) {
#pragma unroll
          for (int j = 0; j < 4; j++) { int r = 16 * w8 + 4 * j + g; dma16(srdW2, r * 768 + ((p ^ (((r & 3) | ((r >> 1) & 4)) << 1)) << 4), M_W2S + (4 * w8 + j) * 1024); }
        }
        asm volatile("s_waitcnt vmcnt(%1)" : "+v"(wv[i]) : "i"(7 - i + (i >= DPOS ? 4 : 0)));
        u32 f = ((2 * i) & 3) | hi | (i & 4);
        u32 ad = M_W2V + (16 * w8 + hi) * 512 + (c & 16) * 16 + (c & 3) * 8 + (((((c >> 2) & 3) * 2) ^ f) << 5);
        *(LAS u32x2*)(ad + i * 1024) = u32x2{__builtin_amdgcn_perm(wv[i][1], wv[i][0], 0x05040100u), __builtin_amdgcn_perm(wv[i][3], wv[i][2], 0x05040100u)};
        *(LAS u32x2*)((ad ^ 32) + i * 1024) = u32x2{__builtin_amdgcn_perm(wv[i][1], wv[i][0], 0x07060302u), __builtin_amdgcn_perm(wv[i][3], wv[i][2], 0x07060302u)};
      }
      asm volatile("" : "+v"(warm) :: "memory");
    } else {
      u32 lx = 0;
      if (w < 4) asm volatile("global_load_ushort %0, %1, off" : "+v"(lx) : "v"(lut_addr(freqs, wrbf, mb, t)));
#pragma unroll
      for (int i = 0; i < 2; i++) { int r = 8 * w + 4 * i + g; dma16(srdS, r * 1024 + ((p ^ (r & 15)) << 4), M_S + (2 * w + i) * 1024); }
#pragma unroll
      for (int i = 0; i < 4; i++) { int k = (4 * w + i + mb) & 31, r = 4 * k + g; dma16(srdW1, r * 256 + ((p ^ (((r & 3) | ((r >> 1) & 4)) << 1)) << 4), M_W1 + k * 1024); }
      if (w < 4) {
        const int row = mb * 12 + (t >> 4);
        const float ld = b2f(LUT_BASE_BITS + row);
        float fc = rbh(0.5f * rbh(rbh(sinq(rbh(rbh(3.141592653589793f * ld) / 5.0f), 1)) + 1.0f));
        asm volatile("s_waitcnt vmcnt(6)" : "+v"(lx), "+v"(fc));
        *(LAS u16*)(M_H + t * 2) = (u16)(w == 3 ? lx : row >= 2 ? pk2(rbh(rbh(sinq(rbh(ld * b2f(lx)), 0)) / ld) * fc, 0.f) : 0u);
        lq = lds128(M_H + w * 128 + (lane & 7) * 16);
        asm volatile("" : "+v"(lq));
      }
      asm volatile("s_waitcnt vmcnt(0)" ::: "memory");
      SBAR();
      u32x4 a[4], b[4][4];
      u32 ab = M_W1 + (8 * g + q) * 256 + ((w ^ sw) << 5) + 8 * pp; OPQ(ab);
#pragma unroll
      for (int kk = 0; kk < 4; kk++) {
        a[kk] = ldstr2(ab + 8192 * kk, 1024);
        u32 bb = M_S + p * 256 + (((4 * kk + g) ^ p) << 4); OPQ(bb);
#pragma unroll
        for (int j = 0; j < 4; j++) b[kk][j] = lds128(bb + 4096 * j);
      }
      __builtin_amdgcn_sched_barrier(0);
      f32x4 hacc[4] = {{0, 0, 0, 0}, {0, 0, 0, 0}, {0, 0, 0, 0}, {0, 0, 0, 0}};
#pragma unroll
      for (int kk = 0; kk < 4; kk++)
#pragma unroll
        for (int j = 0; j < 4; j++) hacc[j] = mfma(a[kk], b[kk][j], hacc[j]);
      __builtin_amdgcn_sched_barrier(0);
      u32 hb = M_H + p * 256 + (((2 * w + (g >> 1)) ^ p) << 4) + 8 * (g & 1); OPQ(hb);
#pragma unroll
      for (int j = 0; j < 4; j++) {
        float y[4];
#pragma unroll
        for (int i = 0; i < 4; i++) { float x = rbh(hacc[j][i]); y[i] = x * __builtin_amdgcn_rcpf(1.0f + __builtin_amdgcn_exp2f(-1.4426950408889634f * x)); }
        *(LAS u32x2*)(hb + 4096 * j) = u32x2{pk2(y[0], y[1]), pk2(y[2], y[3])};
      }
    }
    u32 vv[3][4];
    {
      const u32 vo = (16 * nt + 4 * g) * 1024 + 64 * u + 4 * p;
#pragma unroll
      for (int i = 0; i < 4; i++)
#pragma unroll
        for (int x = 0; x < 3; x++) asm volatile("buffer_load_dword %0, %1, %2, 0 offen offset:%3" : "=v"(vv[x][i]) : "v"(vo), "s"(srdS), "i"(i * 1024 + 256 * (x + 1)) : "memory");
    }
    u32 snap = lds32(M_S);
    asm volatile("s_waitcnt lgkmcnt(0)\n\ts_barrier" : "+v"(snap) :: "memory");
    const float tiny = 1e-36f;
    f32x4 sa[2], va[4];
    {
      u32x4 hf4[4], bw[4][4];
      u32 wb0 = M_W2V + (8 * g + q) * 512 + (((2 * u) ^ sw) << 5) + 8 * pp, wb1 = wb0 ^ 32; OPQ(wb0); OPQ(wb1);
#pragma unroll
      for (int kk = 0; kk < 4; kk++) {
        hf4[kk] = lds128(M_H + (16 * nt + p) * 256 + (((4 * kk + g) ^ p) << 4));
#pragma unroll
        for (int j = 0; j < 4; j++) bw[kk][j] = ldstr2(((j & 1) ? wb1 : wb0) + 16384 * kk + (j >> 1) * 256, 2048);
      }
      u32 st = M_W1 + w * 1024;
      __builtin_amdgcn_sched_barrier(0);
#pragma unroll
      for (int j = 0; j < 4; j++) va[j] = f32x4{tiny, tiny, tiny, tiny};
#pragma unroll
      for (int kk = 0; kk < 4; kk++)
#pragma unroll
        for (int j = 0; j < 4; j++) va[j] = mfma(hf4[kk], bw[kk][j], va[j]);
      __builtin_amdgcn_sched_barrier(0);
      if (w >= 8) { asm volatile("s_waitcnt vmcnt(0)" ::: "memory"); if (lane == 0) ldsadd(M_S, 1u); }
      auto rsV = __builtin_amdgcn_make_buffer_rsrc((void*)(tab + ((size_t)u * N + nb) * 256), 0, nv * 256, 0x00020000);
      sa[0] = sa[1] = f32x4{tiny, tiny, tiny, tiny};
#pragma unroll
      for (int i = 0; i < 4; i++) {
        asm volatile("s_waitcnt vmcnt(%3)" : "+v"(vv[0][i]), "+v"(vv[1][i]), "+v"(vv[2][i]) : "i"(9 - 2 * i));
        float f0 = va[0][i], f1 = va[1][i];
        u32x4 o = dpack(fmaf(f0, b2f(vv[1][i]), tiny), fmaf(f0, b2f(vv[0][i]), tiny), fmaf(f1, h2f(vv[1][i]), tiny), fmaf(f1, h2f(vv[0][i]), tiny),
                        fmaf(f0, b2f(vv[2][i]), tiny), va[2][i], fmaf(f1, h2f(vv[2][i]), tiny), va[3][i]);
        __builtin_amdgcn_raw_buffer_store_b128(o, rsV, (16 * nt + 4 * g + i) * 256 + 16 * p, 0, 16);
        __builtin_amdgcn_sched_barrier(0);
      }
      while (*(volatile LAS u32*)M_S - snap != 8u) __builtin_amdgcn_s_sleep(1);
      asm volatile("" ::: "memory");
      u32 sb = M_W2S + (8 * g + q) * 256 + (((4 * u + pp) ^ (sw << 1)) << 4); OPQ(sb);
      u32x4 aw[4][2];
#pragma unroll
      for (int kk = 0; kk < 4; kk++)
#pragma unroll
        for (int j = 0; j < 2; j++) aw[kk][j] = ldstr2(sb + 8192 * kk + 8 * j, 1024);
      __builtin_amdgcn_sched_barrier(0);
#pragma unroll
      for (int kk = 0; kk < 4; kk++) { sa[0] = mfma(aw[kk][0], hf4[kk], sa[0]); sa[1] = mfma(aw[kk][1], hf4[kk], sa[1]); }
      u32x4 o = dpack(sa[0][2], sa[0][0], sa[0][3], sa[0][1], sa[1][0], sa[1][2], sa[1][1], sa[1][3]);
      *(LAS u32x4*)(st + p * 64 + ((g ^ (p >> 2)) << 4)) = o;
      o = lds128(st + (lane >> 2) * 64 + (((lane & 3) ^ (lane >> 4)) << 4));
      auto rsT = __builtin_amdgcn_make_buffer_rsrc((void*)(tab + ((size_t)4 * N + nb) * 256), 0, nv * 256, 0x00020000);
      __builtin_amdgcn_raw_buffer_store_b128(o, rsT, (16 * nt + (lane >> 2)) * 256 + 64 * u + 16 * (lane & 3), 0, 16);
    }
    if (w < 4) {
      const bool isw = w == 3;
      auto rsL = __builtin_amdgcn_make_buffer_rsrc((void*)(isw ? wfrag : ws), 0, isw ? 12288 : LUT_BYTES, 0x00020000);
      if (lane < 8) __builtin_amdgcn_raw_buffer_store_b128(lq, rsL, (isw ? mb * 128 : mb * 384 + w * 128) + lane * 16, 0, 16);
    }
  } else {
    ARGS("bin");
    const int pr = blockIdx.x;
    const int ebase = pr * EP, ecnt = __builtin_amdgcn_readfirstlane(max(0, min(EP, E - ebase)));
    const u32x4 srdE = mksrd(src + ebase, 0, ecnt * 4, 0x00020000u), srdD = mksrd(dst + ebase, 0, ecnt * 4, 0x00020000u);
    const u32x4 srdP = mksrd(pos, 0, N * 6, 0x00020000u);
    const int rot = (pr >> 3) * 10 + w;
    u32x4 s4[2], d4[2];
    { int i = rot % 80; if (i < 74) dma16(srdP, i * 1024 + lane * 16, B_POS + i * 1024); }
    asm volatile("buffer_load_dwordx4 v[32:35], %2, %3, 0 offen\n\tbuffer_load_dwordx4 v[36:39], %2, %4, 0 offen" : "=&{v[32:35]}"(s4[0]), "=&{v[36:39]}"(d4[0]) : "v"(t * 16), "s"(srdE), "s"(srdD));
    if (t < 66) st32(B_HIST + t * 4, t < 64 ? B_R8 + t * RCAP * 8 : 0);
    SBAR();
#pragma unroll
    for (int k = 1; k < 5; k++) { int i = (rot + 16 * k) % 80; if (i < 74) dma16(srdP, i * 1024 + lane * 16, B_POS + i * 1024); }
    SBAR();
    asm volatile("buffer_load_dwordx4 v[40:43], %2, %3, 0 offen\n\tbuffer_load_dwordx4 v[44:47], %2, %4, 0 offen" : "=&{v[40:43]}"(s4[1]), "=&{v[44:47]}"(d4[1]) : "v"(16384 + t * 16), "s"(srdE), "s"(srdD));
    asm volatile("s_waitcnt vmcnt(2)" ::: "memory");
    BAR();
    u32 dlo = ~0u;
    if (t * 4 < ecnt)
      asm volatile(
  "s_mov_b64 s[78:79], exec\n"
  "s_movk_i32 s80, 0xffcf\n"
  "s_mov_b32 s81, 0x40a00000\n"
  "v_mul_u32_u24 v48, 6, v32\n"
  "v_and_b32 v48, -4, v48\n"
  "ds_read2_b32 v[48:49], v48 offset0:128 offset1:129\n"
  "v_mul_u32_u24 v56, 6, v36\n"
  "v_and_b32 v56, -4, v56\n"
  "ds_read2_b32 v[56:57], v56 offset0:128 offset1:129\n"
  "v_mul_u32_u24 v50, 6, v33\n"
  "v_and_b32 v50, -4, v50\n"
  "ds_read2_b32 v[50:51], v50 offset0:128 offset1:129\n"
  "v_mul_u32_u24 v58, 6, v37\n"
  "v_and_b32 v58, -4, v58\n"
  "ds_read2_b32 v[58:59], v58 offset0:128 offset1:129\n"
  "v_mul_u32_u24 v52, 6, v34\n"
  "v_and_b32 v52, -4, v52\n"
  "ds_read2_b32 v[52:53], v52 offset0:128 offset1:129\n"
  "v_mul_u32_u24 v60, 6, v38\n"
  "v_and_b32 v60, -4, v60\n"
  "ds_read2_b32 v[60:61], v60 offset0:128 offset1:129\n"
  "v_mul_u32_u24 v54, 6, v35\n"
  "v_and_b32 v54, -4, v54\n"
  "ds_read2_b32 v[54:55], v54 offset0:128 offset1:129\n"
  "v_mul_u32_u24 v62, 6, v39\n"
  "v_and_b32 v62, -4, v62\n"
  "ds_read2_b32 v[62:63], v62 offset0:128 offset1:129\n"
  "v_mov_b32 v104, 8\n"
  "v_mul_u32_u24 v72, 0x5398, v36\n"
  "v_mul_u32_u24 v73, 0x5398, v37\n"
  "v_mul_u32_u24 v74, 0x5398, v38\n"
  "v_mul_u32_u24 v75, 0x5398, v39\n"
  "v_lshlrev_b32 v64, 4, v32\n"
  "v_lshlrev_b32 v65, 4, v33\n"
  "v_lshlrev_b32 v66, 4, v34\n"
  "v_lshlrev_b32 v67, 4, v35\n"
  "v_lshrrev_b32 v72, 20, v72\n"
  "v_lshrrev_b32 v73, 20, v73\n"
  "v_lshrrev_b32 v74, 20, v74\n"
  "v_lshrrev_b32 v75, 20, v75\n"
  "v_lshlrev_b32 v68, 4, v36\n"
  "v_lshlrev_b32 v69, 4, v37\n"
  "v_lshlrev_b32 v70, 4, v38\n"
  "v_lshlrev_b32 v71, 4, v39\n"
  "v_and_b32 v72, -4, v72\n"
  "v_and_b32 v73, -4, v73\n"
  "v_and_b32 v74, -4, v74\n"
  "v_and_b32 v75, -4, v75\n"
  "v_mad_i32_i24 v76, v72, s80, v36\n"
  "v_mad_i32_i24 v77, v73, s80, v37\n"
  "v_mad_i32_i24 v78, v74, s80, v38\n"
  "v_mad_i32_i24 v79, v75, s80, v39\n"
  "v_lshl_add_u32 v76, v32, 8, v76\n"
  "v_lshl_add_u32 v77, v33, 8, v77\n"
  "v_lshl_add_u32 v78, v34, 8, v78\n"
  "v_lshl_add_u32 v79, v35, 8, v79\n"
  "s_waitcnt lgkmcnt(6)\n"
  "v_alignbit_b32 v48, v49, v48, v64\n"
  "v_alignbit_b32 v56, v57, v56, v68\n"
  "v_lshrrev_b32_sdwa v49, v64, v49 dst_sel:WORD_1 dst_unused:UNUSED_PAD src0_sel:DWORD src1_sel:DWORD\n"
  "v_lshrrev_b32_sdwa v57, v68, v57 dst_sel:WORD_1 dst_unused:UNUSED_PAD src0_sel:DWORD src1_sel:DWORD\n"
  "v_lshlrev_b32 v64, 16, v48\n"
  "v_lshlrev_b32 v68, 16, v56\n"
  "v_and_b32 v48, 0xffff0000, v48\n"
  "v_and_b32 v56, 0xffff0000, v56\n"
  "v_sub_f32 v80, v64, v68\n"
  "v_pk_add_f32 v[82:83], v[48:49], v[56:57] neg_lo:[0,1] neg_hi:[0,1]\n"
  "s_waitcnt lgkmcnt(4)\n"
  "v_alignbit_b32 v50, v51, v50, v65\n"
  "v_alignbit_b32 v58, v59, v58, v69\n"
  "v_lshrrev_b32_sdwa v51, v65, v51 dst_sel:WORD_1 dst_unused:UNUSED_PAD src0_sel:DWORD src1_sel:DWORD\n"
  "v_lshrrev_b32_sdwa v59, v69, v59 dst_sel:WORD_1 dst_unused:UNUSED_PAD src0_sel:DWORD src1_sel:DWORD\n"
  "v_lshlrev_b32 v65, 16, v50\n"
  "v_lshlrev_b32 v69, 16, v58\n"
  "v_and_b32 v50, 0xffff0000, v50\n"
  "v_and_b32 v58, 0xffff0000, v58\n"
  "v_sub_f32 v86, v65, v69\n"
  "v_pk_add_f32 v[88:89], v[50:51], v[58:59] neg_lo:[0,1] neg_hi:[0,1]\n"
  "s_waitcnt lgkmcnt(2)\n"
  "v_alignbit_b32 v52, v53, v52, v66\n"
  "v_alignbit_b32 v60, v61, v60, v70\n"
  "v_lshrrev_b32_sdwa v53, v66, v53 dst_sel:WORD_1 dst_unused:UNUSED_PAD src0_sel:DWORD src1_sel:DWORD\n"
  "v_lshrrev_b32_sdwa v61, v70, v61 dst_sel:WORD_1 dst_unused:UNUSED_PAD src0_sel:DWORD src1_sel:DWORD\n"
  "v_lshlrev_b32 v66, 16, v52\n"
  "v_lshlrev_b32 v70, 16, v60\n"
  "v_and_b32 v52, 0xffff0000, v52\n"
  "v_and_b32 v60, 0xffff0000, v60\n"
  "v_sub_f32 v92, v66, v70\n"
  "v_pk_add_f32 v[94:95], v[52:53], v[60:61] neg_lo:[0,1] neg_hi:[0,1]\n"
  "s_waitcnt lgkmcnt(0)\n"
  "v_alignbit_b32 v54, v55, v54, v67\n"
  "v_alignbit_b32 v62, v63, v62, v71\n"
  "v_lshrrev_b32_sdwa v55, v67, v55 dst_sel:WORD_1 dst_unused:UNUSED_PAD src0_sel:DWORD src1_sel:DWORD\n"
  "v_lshrrev_b32_sdwa v63, v71, v63 dst_sel:WORD_1 dst_unused:UNUSED_PAD src0_sel:DWORD src1_sel:DWORD\n"
  "v_lshlrev_b32 v67, 16, v54\n"
  "v_lshlrev_b32 v71, 16, v62\n"
  "v_and_b32 v54, 0xffff0000, v54\n"
  "v_and_b32 v62, 0xffff0000, v62\n"
  "v_sub_f32 v98, v67, v71\n"
  "v_pk_add_f32 v[100:101], v[54:55], v[62:63] neg_lo:[0,1] neg_hi:[0,1]\n"
  "v_cvt_pk_bf16_f32 v80, 0, v80\n"
  "v_cvt_pk_bf16_f32 v82, 0, v82\n"
  "v_cvt_pk_bf16_f32 v83, 0, v83\n"
  "v_cvt_pk_bf16_f32 v86, 0, v86\n"
  "v_cvt_pk_bf16_f32 v88, 0, v88\n"
  "v_cvt_pk_bf16_f32 v89, 0, v89\n"
  "v_cvt_pk_bf16_f32 v92, 0, v92\n"
  "v_cvt_pk_bf16_f32 v94, 0, v94\n"
  "v_cvt_pk_bf16_f32 v95, 0, v95\n"
  "v_cvt_pk_bf16_f32 v98, 0, v98\n"
  "v_cvt_pk_bf16_f32 v100, 0, v100\n"
  "v_cvt_pk_bf16_f32 v101, 0, v101\n"
  "v_mul_f32 v81, v80, v80\n"
  "v_pk_mul_f32 v[84:85], v[82:83], v[82:83]\n"
  "v_mul_f32 v87, v86, v86\n"
  "v_pk_mul_f32 v[90:91], v[88:89], v[88:89]\n"
  "v_mul_f32 v93, v92, v92\n"
  "v_pk_mul_f32 v[96:97], v[94:95], v[94:95]\n"
  "v_mul_f32 v99, v98, v98\n"
  "v_pk_mul_f32 v[102:103], v[100:101], v[100:101]\n"
  "v_cvt_pk_bf16_f32 v81, 0, v81\n"
  "v_cvt_pk_bf16_f32 v84, 0, v84\n"
  "v_cvt_pk_bf16_f32 v85, 0, v85\n"
  "v_cvt_pk_bf16_f32 v87, 0, v87\n"
  "v_cvt_pk_bf16_f32 v90, 0, v90\n"
  "v_cvt_pk_bf16_f32 v91, 0, v91\n"
  "v_cvt_pk_bf16_f32 v93, 0, v93\n"
  "v_cvt_pk_bf16_f32 v96, 0, v96\n"
  "v_cvt_pk_bf16_f32 v97, 0, v97\n"
  "v_cvt_pk_bf16_f32 v99, 0, v99\n"
  "v_cvt_pk_bf16_f32 v102, 0, v102\n"
  "v_cvt_pk_bf16_f32 v103, 0, v103\n"
  "v_add_f32 v81, v81, v84\n"
  "v_add_f32 v87, v87, v90\n"
  "v_add_f32 v93, v93, v96\n"
  "v_add_f32 v99, v99, v102\n"
  "v_add_f32 v81, v81, v85\n"
  "v_add_f32 v87, v87, v91\n"
  "v_add_f32 v93, v93, v97\n"
  "v_add_f32 v99, v99, v103\n"
  "v_cvt_pk_bf16_f32 v81, 0, v81\n"
  "v_cvt_pk_bf16_f32 v87, 0, v87\n"
  "v_cvt_pk_bf16_f32 v93, 0, v93\n"
  "v_cvt_pk_bf16_f32 v99, 0, v99\n"
  "v_add_f32 v81, 0x322bcc77, v81\n"
  "v_add_f32 v87, 0x322bcc77, v87\n"
  "v_add_f32 v93, 0x322bcc77, v93\n"
  "v_add_f32 v99, 0x322bcc77, v99\n"
  "v_cvt_pk_bf16_f32 v81, 0, v81\n"
  "v_cvt_pk_bf16_f32 v87, 0, v87\n"
  "v_cvt_pk_bf16_f32 v93, 0, v93\n"
  "v_cvt_pk_bf16_f32 v99, 0, v99\n"
  "v_sqrt_f32 v81, v81\n"
  "v_sqrt_f32 v87, v87\n"
  "v_sqrt_f32 v93, v93\n"
  "v_sqrt_f32 v99, v99\n"
  "v_cvt_pk_bf16_f32 v81, 0, v81\n"
  "v_cvt_pk_bf16_f32 v87, 0, v87\n"
  "v_cvt_pk_bf16_f32 v93, 0, v93\n"
  "v_cvt_pk_bf16_f32 v99, 0, v99\n"
  "v_cmp_gt_f32 s[70:71], s81, v81\n"
  "v_cmp_gt_f32 s[72:73], s81, v87\n"
  "v_cmp_gt_f32 s[74:75], s81, v93\n"
  "v_cmp_gt_f32 s[76:77], s81, v99\n"
  "v_rcp_f32 v84, v81\n"
  "v_rcp_f32 v90, v87\n"
  "v_rcp_f32 v96, v93\n"
  "v_rcp_f32 v102, v99\n"
  "v_subrev_u32 v64, 0x38f00000, v81\n"
  "v_subrev_u32 v65, 0x38f00000, v87\n"
  "v_subrev_u32 v66, 0x38f00000, v93\n"
  "v_subrev_u32 v67, 0x38f00000, v99\n"
  "v_min3_u32 v105, v105, v64, v65\n"
  "v_min3_u32 v105, v105, v66, v67\n"
  "s_mov_b64 exec, s[70:71]\n"
  "ds_add_rtn_u32 v72, v72, v104\n"
  "s_mov_b64 exec, s[72:73]\n"
  "ds_add_rtn_u32 v73, v73, v104\n"
  "s_mov_b64 exec, s[74:75]\n"
  "ds_add_rtn_u32 v74, v74, v104\n"
  "s_mov_b64 exec, s[76:77]\n"
  "ds_add_rtn_u32 v75, v75, v104\n"
  "s_mov_b64 exec, s[78:79]\n"
  "v_mul_f32 v80, v80, v84\n"
  "v_pk_mul_f32 v[82:83], v[82:83], v[84:85] op_sel_hi:[1,0]\n"
  "v_mul_f32 v86, v86, v90\n"
  "v_pk_mul_f32 v[88:89], v[88:89], v[90:91] op_sel_hi:[1,0]\n"
  "v_mul_f32 v92, v92, v96\n"
  "v_pk_mul_f32 v[94:95], v[94:95], v[96:97] op_sel_hi:[1,0]\n"
  "v_mul_f32 v98, v98, v102\n"
  "v_pk_mul_f32 v[100:101], v[100:101], v[102:103] op_sel_hi:[1,0]\n"
  "v_cvt_pk_bf16_f32 v80, v80, v82\n"
  "v_cvt_pk_bf16_f32 v81, v83, v81\n"
  "v_cvt_pk_bf16_f32 v86, v86, v88\n"
  "v_cvt_pk_bf16_f32 v87, v89, v87\n"
  "v_cvt_pk_bf16_f32 v92, v92, v94\n"
  "v_cvt_pk_bf16_f32 v93, v95, v93\n"
  "v_cvt_pk_bf16_f32 v98, v98, v100\n"
  "v_cvt_pk_bf16_f32 v99, v101, v99\n"
  "s_waitcnt lgkmcnt(0)\n"
  "v_lshrrev_b32 v82, 1, v72\n"
  "v_lshrrev_b32 v88, 1, v73\n"
  "v_lshrrev_b32 v94, 1, v74\n"
  "v_lshrrev_b32 v100, 1, v75\n"
  "s_mov_b64 exec, s[70:71]\n"
  "ds_write_b64 v72, v[80:81]\n"
  "ds_write_b32 v82, v76 offset:23808\n"
  "s_mov_b64 exec, s[72:73]\n"
  "ds_write_b64 v73, v[86:87]\n"
  "ds_write_b32 v88, v77 offset:23808\n"
  "s_mov_b64 exec, s[74:75]\n"
  "ds_write_b64 v74, v[92:93]\n"
  "ds_write_b32 v94, v78 offset:23808\n"
  "s_mov_b64 exec, s[76:77]\n"
  "ds_write_b64 v75, v[98:99]\n"
  "ds_write_b32 v100, v79 offset:23808\n"
  "s_mov_b64 exec, s[78:79]\n"
          : "+{v105}"(dlo) : "{v[32:35]}"(s4[0]), "{v[36:39]}"(d4[0]) : "v48", "v49", "v50", "v51", "v52", "v53", "v54", "v55", "v56", "v57", "v58", "v59", "v60", "v61", "v62", "v63", "v64", "v65", "v66", "v67", "v68", "v69", "v70", "v71", "v72", "v73", "v74", "v75", "v76", "v77", "v78", "v79", "v80", "v81", "v82", "v83", "v84", "v85", "v86", "v87", "v88", "v89", "v90", "v91", "v92", "v93", "v94", "v95", "v96", "v97", "v98", "v99", "v100", "v101", "v102", "v103", "v104", "s70", "s71", "s72", "s73", "s74", "s75", "s76", "s77", "s78", "s79", "s80", "s81", "memory");
    asm volatile("s_waitcnt vmcnt(0)" ::: "memory");
    if (4096 + t * 4 < ecnt)
      asm volatile(
  "s_mov_b64 s[78:79], exec\n"
  "s_movk_i32 s80, 0xffcf\n"
  "s_mov_b32 s81, 0x40a00000\n"
  "v_mul_u32_u24 v48, 6, v40\n"
  "v_and_b32 v48, -4, v48\n"
  "ds_read2_b32 v[48:49], v48 offset0:128 offset1:129\n"
  "v_mul_u32_u24 v56, 6, v44\n"
  "v_and_b32 v56, -4, v56\n"
  "ds_read2_b32 v[56:57], v56 offset0:128 offset1:129\n"
  "v_mul_u32_u24 v50, 6, v41\n"
  "v_and_b32 v50, -4, v50\n"
  "ds_read2_b32 v[50:51], v50 offset0:128 offset1:129\n"
  "v_mul_u32_u24 v58, 6, v45\n"
  "v_and_b32 v58, -4, v58\n"
  "ds_read2_b32 v[58:59], v58 offset0:128 offset1:129\n"
  "v_mul_u32_u24 v52, 6, v42\n"
  "v_and_b32 v52, -4, v52\n"
  "ds_read2_b32 v[52:53], v52 offset0:128 offset1:129\n"
  "v_mul_u32_u24 v60, 6, v46\n"
  "v_and_b32 v60, -4, v60\n"
  "ds_read2_b32 v[60:61], v60 offset0:128 offset1:129\n"
  "v_mul_u32_u24 v54, 6, v43\n"
  "v_and_b32 v54, -4, v54\n"
  "ds_read2_b32 v[54:55], v54 offset0:128 offset1:129\n"
  "v_mul_u32_u24 v62, 6, v47\n"
  "v_and_b32 v62, -4, v62\n"
  "ds_read2_b32 v[62:63], v62 offset0:128 offset1:129\n"
  "v_mov_b32 v104, 8\n"
  "v_mul_u32_u24 v72, 0x5398, v44\n"
  "v_mul_u32_u24 v73, 0x5398, v45\n"
  "v_mul_u32_u24 v74, 0x5398, v46\n"
  "v_mul_u32_u24 v75, 0x5398, v47\n"
  "v_lshlrev_b32 v64, 4, v40\n"
  "v_lshlrev_b32 v65, 4, v41\n"
  "v_lshlrev_b32 v66, 4, v42\n"
  "v_lshlrev_b32 v67, 4, v43\n"
  "v_lshrrev_b32 v72, 20, v72\n"
  "v_lshrrev_b32 v73, 20, v73\n"
  "v_lshrrev_b32 v74, 20, v74\n"
  "v_lshrrev_b32 v75, 20, v75\n"
  "v_lshlrev_b32 v68, 4, v44\n"
  "v_lshlrev_b32 v69, 4, v45\n"
  "v_lshlrev_b32 v70, 4, v46\n"
  "v_lshlrev_b32 v71, 4, v47\n"
  "v_and_b32 v72, -4, v72\n"
  "v_and_b32 v73, -4, v73\n"
  "v_and_b32 v74, -4, v74\n"
  "v_and_b32 v75, -4, v75\n"
  "v_mad_i32_i24 v76, v72, s80, v44\n"
  "v_mad_i32_i24 v77, v73, s80, v45\n"
  "v_mad_i32_i24 v78, v74, s80, v46\n"
  "v_mad_i32_i24 v79, v75, s80, v47\n"
  "v_lshl_add_u32 v76, v40, 8, v76\n"
  "v_lshl_add_u32 v77, v41, 8, v77\n"
  "v_lshl_add_u32 v78, v42, 8, v78\n"
  "v_lshl_add_u32 v79, v43, 8, v79\n"
  "s_waitcnt lgkmcnt(6)\n"
  "v_alignbit_b32 v48, v49, v48, v64\n"
  "v_alignbit_b32 v56, v57, v56, v68\n"
  "v_lshrrev_b32_sdwa v49, v64, v49 dst_sel:WORD_1 dst_unused:UNUSED_PAD src0_sel:DWORD src1_sel:DWORD\n"
  "v_lshrrev_b32_sdwa v57, v68, v57 dst_sel:WORD_1 dst_unused:UNUSED_PAD src0_sel:DWORD src1_sel:DWORD\n"
  "v_lshlrev_b32 v64, 16, v48\n"
  "v_lshlrev_b32 v68, 16, v56\n"
  "v_and_b32 v48, 0xffff0000, v48\n"
  "v_and_b32 v56, 0xffff0000, v56\n"
  "v_sub_f32 v80, v64, v68\n"
  "v_pk_add_f32 v[82:83], v[48:49], v[56:57] neg_lo:[0,1] neg_hi:[0,1]\n"
  "s_waitcnt lgkmcnt(4)\n"
  "v_alignbit_b32 v50, v51, v50, v65\n"
  "v_alignbit_b32 v58, v59, v58, v69\n"
  "v_lshrrev_b32_sdwa v51, v65, v51 dst_sel:WORD_1 dst_unused:UNUSED_PAD src0_sel:DWORD src1_sel:DWORD\n"
  "v_lshrrev_b32_sdwa v59, v69, v59 dst_sel:WORD_1 dst_unused:UNUSED_PAD src0_sel:DWORD src1_sel:DWORD\n"
  "v_lshlrev_b32 v65, 16, v50\n"
  "v_lshlrev_b32 v69, 16, v58\n"
  "v_and_b32 v50, 0xffff0000, v50\n"
  "v_and_b32 v58, 0xffff0000, v58\n"
  "v_sub_f32 v86, v65, v69\n"
  "v_pk_add_f32 v[88:89], v[50:51], v[58:59] neg_lo:[0,1] neg_hi:[0,1]\n"
  "s_waitcnt lgkmcnt(2)\n"
  "v_alignbit_b32 v52, v53, v52, v66\n"
  "v_alignbit_b32 v60, v61, v60, v70\n"
  "v_lshrrev_b32_sdwa v53, v66, v53 dst_sel:WORD_1 dst_unused:UNUSED_PAD src0_sel:DWORD src1_sel:DWORD\n"
  "v_lshrrev_b32_sdwa v61, v70, v61 dst_sel:WORD_1 dst_unused:UNUSED_PAD src0_sel:DWORD src1_sel:DWORD\n"
  "v_lshlrev_b32 v66, 16, v52\n"
  "v_lshlrev_b32 v70, 16, v60\n"
  "v_and_b32 v52, 0xffff0000, v52\n"
  "v_and_b32 v60, 0xffff0000, v60\n"
  "v_sub_f32 v92, v66, v70\n"
  "v_pk_add_f32 v[94:95], v[52:53], v[60:61] neg_lo:[0,1] neg_hi:[0,1]\n"
  "s_waitcnt lgkmcnt(0)\n"
  "v_alignbit_b32 v54, v55, v54, v67\n"
  "v_alignbit_b32 v62, v63, v62, v71\n"
  "v_lshrrev_b32_sdwa v55, v67, v55 dst_sel:WORD_1 dst_unused:UNUSED_PAD src0_sel:DWORD src1_sel:DWORD\n"
  "v_lshrrev_b32_sdwa v63, v71, v63 dst_sel:WORD_1 dst_unused:UNUSED_PAD src0_sel:DWORD src1_sel:DWORD\n"
  "v_lshlrev_b32 v67, 16, v54\n"
  "v_lshlrev_b32 v71, 16, v62\n"
  "v_and_b32 v54, 0xffff0000, v54\n"
  "v_and_b32 v62, 0xffff0000, v62\n"
  "v_sub_f32 v98, v67, v71\n"
  "v_pk_add_f32 v[100:101], v[54:55], v[62:63] neg_lo:[0,1] neg_hi:[0,1]\n"
  "v_cvt_pk_bf16_f32 v80, 0, v80\n"
  "v_cvt_pk_bf16_f32 v82, 0, v82\n"
  "v_cvt_pk_bf16_f32 v83, 0, v83\n"
  "v_cvt_pk_bf16_f32 v86, 0, v86\n"
  "v_cvt_pk_bf16_f32 v88, 0, v88\n"
  "v_cvt_pk_bf16_f32 v89, 0, v89\n"
  "v_cvt_pk_bf16_f32 v92, 0, v92\n"
  "v_cvt_pk_bf16_f32 v94, 0, v94\n"
  "v_cvt_pk_bf16_f32 v95, 0, v95\n"
  "v_cvt_pk_bf16_f32 v98, 0, v98\n"
  "v_cvt_pk_bf16_f32 v100, 0, v100\n"
  "v_cvt_pk_bf16_f32 v101, 0, v101\n"
  "v_mul_f32 v81, v80, v80\n"
  "v_pk_mul_f32 v[84:85], v[82:83], v[82:83]\n"
  "v_mul_f32 v87, v86, v86\n"
  "v_pk_mul_f32 v[90:91], v[88:89], v[88:89]\n"
  "v_mul_f32 v93, v92, v92\n"
  "v_pk_mul_f32 v[96:97], v[94:95], v[94:95]\n"
  "v_mul_f32 v99, v98, v98\n"
  "v_pk_mul_f32 v[102:103], v[100:101], v[100:101]\n"
  "v_cvt_pk_bf16_f32 v81, 0, v81\n"
  "v_cvt_pk_bf16_f32 v84, 0, v84\n"
  "v_cvt_pk_bf16_f32 v85, 0, v85\n"
  "v_cvt_pk_bf16_f32 v87, 0, v87\n"
  "v_cvt_pk_bf16_f32 v90, 0, v90\n"
  "v_cvt_pk_bf16_f32 v91, 0, v91\n"
  "v_cvt_pk_bf16_f32 v93, 0, v93\n"
  "v_cvt_pk_bf16_f32 v96, 0, v96\n"
  "v_cvt_pk_bf16_f32 v97, 0, v97\n"
  "v_cvt_pk_bf16_f32 v99, 0, v99\n"
  "v_cvt_pk_bf16_f32 v102, 0, v102\n"
  "v_cvt_pk_bf16_f32 v103, 0, v103\n"
  "v_add_f32 v81, v81, v84\n"
  "v_add_f32 v87, v87, v90\n"
  "v_add_f32 v93, v93, v96\n"
  "v_add_f32 v99, v99, v102\n"
  "v_add_f32 v81, v81, v85\n"
  "v_add_f32 v87, v87, v91\n"
  "v_add_f32 v93, v93, v97\n"
  "v_add_f32 v99, v99, v103\n"
  "v_cvt_pk_bf16_f32 v81, 0, v81\n"
  "v_cvt_pk_bf16_f32 v87, 0, v87\n"
  "v_cvt_pk_bf16_f32 v93, 0, v93\n"
  "v_cvt_pk_bf16_f32 v99, 0, v99\n"
  "v_add_f32 v81, 0x322bcc77, v81\n"
  "v_add_f32 v87, 0x322bcc77, v87\n"
  "v_add_f32 v93, 0x322bcc77, v93\n"
  "v_add_f32 v99, 0x322bcc77, v99\n"
  "v_cvt_pk_bf16_f32 v81, 0, v81\n"
  "v_cvt_pk_bf16_f32 v87, 0, v87\n"
  "v_cvt_pk_bf16_f32 v93, 0, v93\n"
  "v_cvt_pk_bf16_f32 v99, 0, v99\n"
  "v_sqrt_f32 v81, v81\n"
  "v_sqrt_f32 v87, v87\n"
  "v_sqrt_f32 v93, v93\n"
  "v_sqrt_f32 v99, v99\n"
  "v_cvt_pk_bf16_f32 v81, 0, v81\n"
  "v_cvt_pk_bf16_f32 v87, 0, v87\n"
  "v_cvt_pk_bf16_f32 v93, 0, v93\n"
  "v_cvt_pk_bf16_f32 v99, 0, v99\n"
  "v_cmp_gt_f32 s[70:71], s81, v81\n"
  "v_cmp_gt_f32 s[72:73], s81, v87\n"
  "v_cmp_gt_f32 s[74:75], s81, v93\n"
  "v_cmp_gt_f32 s[76:77], s81, v99\n"
  "v_rcp_f32 v84, v81\n"
  "v_rcp_f32 v90, v87\n"
  "v_rcp_f32 v96, v93\n"
  "v_rcp_f32 v102, v99\n"
  "v_subrev_u32 v64, 0x38f00000, v81\n"
  "v_subrev_u32 v65, 0x38f00000, v87\n"
  "v_subrev_u32 v66, 0x38f00000, v93\n"
  "v_subrev_u32 v67, 0x38f00000, v99\n"
  "v_min3_u32 v105, v105, v64, v65\n"
  "v_min3_u32 v105, v105, v66, v67\n"
  "s_mov_b64 exec, s[70:71]\n"
  "ds_add_rtn_u32 v72, v72, v104\n"
  "s_mov_b64 exec, s[72:73]\n"
  "ds_add_rtn_u32 v73, v73, v104\n"
  "s_mov_b64 exec, s[74:75]\n"
  "ds_add_rtn_u32 v74, v74, v104\n"
  "s_mov_b64 exec, s[76:77]\n"
  "ds_add_rtn_u32 v75, v75, v104\n"
  "s_mov_b64 exec, s[78:79]\n"
  "v_mul_f32 v80, v80, v84\n"
  "v_pk_mul_f32 v[82:83], v[82:83], v[84:85] op_sel_hi:[1,0]\n"
  "v_mul_f32 v86, v86, v90\n"
  "v_pk_mul_f32 v[88:89], v[88:89], v[90:91] op_sel_hi:[1,0]\n"
  "v_mul_f32 v92, v92, v96\n"
  "v_pk_mul_f32 v[94:95], v[94:95], v[96:97] op_sel_hi:[1,0]\n"
  "v_mul_f32 v98, v98, v102\n"
  "v_pk_mul_f32 v[100:101], v[100:101], v[102:103] op_sel_hi:[1,0]\n"
  "v_cvt_pk_bf16_f32 v80, v80, v82\n"
  "v_cvt_pk_bf16_f32 v81, v83, v81\n"
  "v_cvt_pk_bf16_f32 v86, v86, v88\n"
  "v_cvt_pk_bf16_f32 v87, v89, v87\n"
  "v_cvt_pk_bf16_f32 v92, v92, v94\n"
  "v_cvt_pk_bf16_f32 v93, v95, v93\n"
  "v_cvt_pk_bf16_f32 v98, v98, v100\n"
  "v_cvt_pk_bf16_f32 v99, v101, v99\n"
  "s_waitcnt lgkmcnt(0)\n"
  "v_lshrrev_b32 v82, 1, v72\n"
  "v_lshrrev_b32 v88, 1, v73\n"
  "v_lshrrev_b32 v94, 1, v74\n"
  "v_lshrrev_b32 v100, 1, v75\n"
  "s_mov_b64 exec, s[70:71]\n"
  "ds_write_b64 v72, v[80:81]\n"
  "ds_write_b32 v82, v76 offset:23808\n"
  "s_mov_b64 exec, s[72:73]\n"
  "ds_write_b64 v73, v[86:87]\n"
  "ds_write_b32 v88, v77 offset:23808\n"
  "s_mov_b64 exec, s[74:75]\n"
  "ds_write_b64 v74, v[92:93]\n"
  "ds_write_b32 v94, v78 offset:23808\n"
  "s_mov_b64 exec, s[76:77]\n"
  "ds_write_b64 v75, v[98:99]\n"
  "ds_write_b32 v100, v79 offset:23808\n"
  "s_mov_b64 exec, s[78:79]\n"
          : "+{v105}"(dlo) : "{v[40:43]}"(s4[1]), "{v[44:47]}"(d4[1]) : "v48", "v49", "v50", "v51", "v52", "v53", "v54", "v55", "v56", "v57", "v58", "v59", "v60", "v61", "v62", "v63", "v64", "v65", "v66", "v67", "v68", "v69", "v70", "v71", "v72", "v73", "v74", "v75", "v76", "v77", "v78", "v79", "v80", "v81", "v82", "v83", "v84", "v85", "v86", "v87", "v88", "v89", "v90", "v91", "v92", "v93", "v94", "v95", "v96", "v97", "v98", "v99", "v100", "v101", "v102", "v103", "v104", "s70", "s71", "s72", "s73", "s74", "s75", "s76", "s77", "s78", "s79", "s80", "s81", "memory");
    if (dlo < ((u32)(COLDP * 32) << 16)) st32(B_HIST + 260, 0x8000u);
    BAR();
    u32 len = (lds32(B_HIST + lane * 4) - (B_R8 + lane * RCAP * 8)) >> 3, novf = 0, cold = lds32(B_HIST + 260);
    if (__builtin_amdgcn_ballot_w64(len > RCAP) != 0 || (ecnt & 3)) {
      BAR();
      if (t < 65) st32(B_HIST + t * 4, 0);
      BAR();
      u32x4* ovf = (u32x4*)(wfrag + W_OVF) + (size_t)pr * PCAP;
#pragma unroll 1
      for (int i = t; i < ecnt; i += 1024) {
        u32 s = src[ebase + i], d = dst[ebase + i];
        Geo e = geom(s, d, ldpos(s), ldpos(d));
        if (e.d < 5.0f) {
          u32 rk = ldsadd(B_HIST + e.b4, 1u);
          if (rk < RCAP) { u32 a = e.b4 * (RCAP / 4) + rk; *(LAS u32x2*)(B_R8 + a * 8) = u32x2{e.r0, e.r1}; st32(B_R4 + a * 4, e.r2); }
          else ovf[ldsadd(B_HIST + 256, 1u)] = u32x4{e.r0, e.r1, e.r2, e.b4 >> 2};
        }
      }
      BAR();
      len = min(lds32(B_HIST + lane * 4), (u32)RCAP);
      novf = lds32(B_HIST + 256); cold = 0x8000u;
    }
    {
      if (w == 0) __builtin_amdgcn_raw_buffer_store_b32(len | cold | (novf << 16), __builtin_amdgcn_make_buffer_rsrc((void*)(wfrag + W_OFFS), 0, 16384, 0x00020000), (pr * 64 + lane) * 4, 0, 16);
      auto rsR = __builtin_amdgcn_make_buffer_rsrc((void*)(wfrag + W_RECS + (size_t)pr * NBINS * RSTR), 0, NBINS * RSTR, 0x00020000);
      u32x4 x[6];
#pragma unroll
      for (int k = 0; k < 2; k++) x[4 + k] = lds128(B_R4 + (4 * w + 2 * k + (lane >> 5)) * RCAP * 4 + (lane & 31) * 16);
#pragma unroll
      for (int k = 0; k < 4; k++) x[k] = lds128(B_R8 + (4 * w + k) * RCAP * 8 + lane * 16);
#pragma unroll
      for (int k = 0; k < 2; k++) {
        u32 ln = max(__builtin_amdgcn_readlane(len, 4 * w + 2 * k), __builtin_amdgcn_readlane(len, 4 * w + 2 * k + 1)) > 96 ? 32 : 24;
        __builtin_amdgcn_raw_buffer_store_b128(x[4 + k], rsR, (lane & 31) < ln ? (4 * w + 2 * k + (lane >> 5)) * RSTR + (lane & 31) * 16 : 0x7ffffff0u, 0, 16);
      }
#pragma unroll
      for (int k = 0; k < 4; k++) {
        u32 ln = (__builtin_amdgcn_readlane(len, 4 * w + k) + 15) & ~15u;
        __builtin_amdgcn_raw_buffer_store_b128(x[k], rsR, lane * 2 < ln ? (4 * w + k) * RSTR + 512 + lane * 16 : 0x7ffffff0u, 0, 16);
      }
    }
  }
  asm volatile("" :: "v"(cpf));
}

#define SCLOB "s52", "s53", "s54", "s55", "s56", "s57", "s58", "scc", "memory"

__device__ __forceinline__ void phase_sca(u32x4 srdT, u32x4 srdS, u32x4 srdO, int w) {
  const int lane = lane_id(), p = lane & 15, g = lane >> 4;
  u32 t0 = L_WS + lane * 16;
  u32 oc = 16 * p, lb = L_REC + (8 * (p >> 2) + ((p >> 1) & 1)) * 8, ls = L_SRC + 32 * g;
  u32 amask = ((g >> 1) == (p & 1)) ? 32u : 0u, aoff = L_LUT + 16 * (g & 1) - amask * LUT_BASE_BITS;
  u32 qaddr = L_MISC + 8, four = 4, tick = L_ITS + 4 * w;
  asm volatile(
  "s_waitcnt lgkmcnt(0)\n"
  "s_barrier\n"
  "ds_read_b32 v124, v123\n"
  "v_mov_b32 v112, v108\n"
  "s_waitcnt lgkmcnt(0)\n"
  "v_readfirstlane_b32 s57, v124\n"
  "s_nop 0\n"
  "s_ashr_i32 s54, s57, 18\n"
  "s_and_b32 s58, s57, 0x3ff\n"
  "s_bfe_u32 s57, s57, 0x8000a\n"
  "s_add_i32 s58, s58, -74432\n"
  "s_lshl_b32 s55, s57, 8\n"
  "v_add_u32 v108, s58, v118\n"
  "s_mov_b32 s56, 1\n"
  "ds_read_b32 v113, v108\n"
  "s_cmp_lt_i32 s54, 0\n"
  "s_cbranch_scc1 Ls_rot_%=\n"
  "s_cmp_gt_i32 s54, 8\n"
  "s_cbranch_scc1 Ls_ntp_%=\n"
  "s_mov_b64 exec, 1\n"
  "ds_add_rtn_u32 v123, v121, v122\n"
  "s_mov_b64 exec, -1\n"
  "Ls_ntp_%=:\n"
  "s_lshr_b32 s57, s55, 1\n"
  "v_add_u32 v108, s57, v118\n"
  "ds_read_b128 v[84:87], v108\n"
  "ds_read_b128 v[88:91], v108 offset:16\n"
  "s_waitcnt lgkmcnt(0)\n"
  "s_cmp_lt_i32 s54, 1\n"
  "s_cbranch_scc1 Ls_gp_%=\n"
  "buffer_load_dwordx4 v[0:3], v84, s[40:43], 0 offen\n"
  "s_cmp_lt_i32 s54, 2\n"
  "s_cbranch_scc1 Ls_gp_%=\n"
  "buffer_load_dwordx4 v[4:7], v85, s[40:43], 0 offen\n"
  "s_cmp_lt_i32 s54, 3\n"
  "s_cbranch_scc1 Ls_gp_%=\n"
  "buffer_load_dwordx4 v[8:11], v86, s[40:43], 0 offen\n"
  "s_cmp_lt_i32 s54, 4\n"
  "s_cbranch_scc1 Ls_gp_%=\n"
  "buffer_load_dwordx4 v[12:15], v87, s[40:43], 0 offen\n"
  "s_cmp_lt_i32 s54, 5\n"
  "s_cbranch_scc1 Ls_gp_%=\n"
  "buffer_load_dwordx4 v[16:19], v88, s[40:43], 0 offen\n"
  "s_cmp_lt_i32 s54, 6\n"
  "s_cbranch_scc1 Ls_gp_%=\n"
  "buffer_load_dwordx4 v[20:23], v89, s[40:43], 0 offen\n"
  "s_cmp_lt_i32 s54, 7\n"
  "s_cbranch_scc1 Ls_gp_%=\n"
  "buffer_load_dwordx4 v[24:27], v90, s[40:43], 0 offen\n"
  "s_cmp_lt_i32 s54, 8\n"
  "s_cbranch_scc1 Ls_gp_%=\n"
  "buffer_load_dwordx4 v[28:31], v91, s[40:43], 0 offen\n"
  "Ls_gp_%=:\n"
  "ds_read_b128 v[92:95], v112 offset:0\n"
  "ds_read_b128 v[96:99], v112 offset:1024\n"
  "ds_read_b128 v[100:103], v112 offset:2048\n"
  "ds_read_b128 v[104:107], v112 offset:3072\n"
  "v_add_u32 v113, v113, v114\n"
  "buffer_load_dwordx4 v[84:87], v113, s[44:47], 0 offen\n"
  "v_add_u32 v108, s55, v117\n"
  "ds_read_u16 v80, v108 offset:6\n"
  "ds_read_u16 v81, v108 offset:22\n"
  "ds_read_u16 v82, v108 offset:38\n"
  "ds_read_u16 v83, v108 offset:54\n"
  "ds_read_b32 v124, v123\n"
  "v_mov_b32 v112, v113\n"
  "s_waitcnt lgkmcnt(1)\n"
  "v_mad_u32_u24 v80, v80, v119, v120\n"
  "ds_read_b128 v[32:35], v80\n"
  "v_mad_u32_u24 v81, v81, v119, v120\n"
  "ds_read_b128 v[36:39], v81\n"
  "v_mad_u32_u24 v82, v82, v119, v120\n"
  "ds_read_b128 v[40:43], v82\n"
  "v_mad_u32_u24 v83, v83, v119, v120\n"
  "ds_read_b128 v[44:47], v83\n"
  "s_waitcnt lgkmcnt(2)\n"
  "v_mfma_f32_16x16x32_bf16 v[48:51], v[32:35], v[92:95], 0\n"
  "v_mfma_f32_16x16x32_bf16 v[52:55], v[32:35], v[96:99], 0\n"
  "v_mfma_f32_16x16x32_bf16 v[56:59], v[32:35], v[100:103], 0\n"
  "v_mfma_f32_16x16x32_bf16 v[32:35], v[32:35], v[104:107], 0\n"
  "v_mfma_f32_16x16x32_bf16 v[60:63], v[36:39], v[92:95], 0\n"
  "v_mfma_f32_16x16x32_bf16 v[64:67], v[36:39], v[96:99], 0\n"
  "v_mfma_f32_16x16x32_bf16 v[68:71], v[36:39], v[100:103], 0\n"
  "v_mfma_f32_16x16x32_bf16 v[36:39], v[36:39], v[104:107], 0\n"
  "s_waitcnt vmcnt(0)\n"
  "s_branch Ls_rot_%=\n"
  "Ls_cont_%=:\n"
  "s_sub_i32 s54, s52, 8\n"
  "s_add_i32 s55, s55, 256\n"
  "s_mov_b32 s56, 0\n"
  "s_branch Ls_join_%=\n"
  "Ls_top_%=:\n"
  "s_waitcnt lgkmcnt(0)\n"
  "s_cmp_gt_i32 s52, 8\n"
  "s_cbranch_scc1 Ls_cont_%=\n"
  "v_readfirstlane_b32 s57, v124\n"
  "s_nop 0\n"
  "s_ashr_i32 s54, s57, 18\n"
  "s_and_b32 s58, s57, 0x3ff\n"
  "s_bfe_u32 s57, s57, 0x8000a\n"
  "s_add_i32 s58, s58, -74432\n"
  "s_lshl_b32 s55, s57, 8\n"
  "v_add_u32 v108, s58, v118\n"
  "s_mov_b32 s56, 1\n"
  "ds_read_b32 v113, v108\n"
  "Ls_join_%=:\n"
  "s_cmp_gt_i32 s54, 8\n"
  "s_cbranch_scc1 Ls_ntl_%=\n"
  "s_mov_b64 exec, 1\n"
  "ds_add_rtn_u32 v123, v121, v122\n"
  "s_mov_b64 exec, -1\n"
  "Ls_ntl_%=:\n"
  "s_setprio 2\n"
  "s_cmp_eq_u32 s53, 0\n"
  "s_cbranch_scc1 Ls_nf_%=\n"
  "s_waitcnt vmcnt(1)\n"
  "v_lshlrev_b32 v72, 16, v84\n"
  "v_and_b32 v73, 0xffff0000, v84\n"
  "v_lshlrev_b32 v74, 16, v85\n"
  "v_and_b32 v75, 0xffff0000, v85\n"
  "v_lshlrev_b32 v76, 16, v86\n"
  "v_and_b32 v77, 0xffff0000, v86\n"
  "v_lshlrev_b32 v78, 16, v87\n"
  "v_and_b32 v79, 0xffff0000, v87\n"
  "s_lshr_b32 s57, s55, 1\n"
  "v_add_u32 v108, s57, v118\n"
  "ds_read_b128 v[84:87], v108\n"
  "ds_read_b128 v[88:91], v108 offset:16\n"
  "s_cmp_lt_i32 s52, 1\n"
  "s_cbranch_scc1 Ls_je_%=\n"
  "s_branch Ls_j0_%=\n"
  "Ls_nf_%=:\n"
  "s_lshr_b32 s57, s55, 1\n"
  "v_add_u32 v108, s57, v118\n"
  "ds_read_b128 v[84:87], v108\n"
  "ds_read_b128 v[88:91], v108 offset:16\n"
  "s_waitcnt vmcnt(0)\n"
  "Ls_j0_%=:\n"
  "v_lshlrev_b64 v[80:81], 16, v[0:1]\n"
  "v_lshlrev_b64 v[82:83], 16, v[2:3]\n"
  "v_pk_fma_f32 v[72:73], v[0:1], v[48:49], v[72:73]\n"
  "v_pk_fma_f32 v[78:79], v[2:3], v[32:33], v[78:79]\n"
  "v_pk_fma_f32 v[74:75], v[80:81], v[52:53], v[74:75]\n"
  "v_pk_fma_f32 v[76:77], v[82:83], v[56:57], v[76:77]\n"
  "s_cmp_lt_i32 s52, 2\n"
  "s_cbranch_scc1 Ls_je_%=\n"
  "v_lshlrev_b64 v[80:81], 16, v[4:5]\n"
  "v_lshlrev_b64 v[82:83], 16, v[6:7]\n"
  "v_pk_fma_f32 v[72:73], v[4:5], v[50:51], v[72:73]\n"
  "v_pk_fma_f32 v[78:79], v[6:7], v[34:35], v[78:79]\n"
  "v_pk_fma_f32 v[74:75], v[80:81], v[54:55], v[74:75]\n"
  "v_pk_fma_f32 v[76:77], v[82:83], v[58:59], v[76:77]\n"
  "s_cmp_lt_i32 s52, 3\n"
  "s_cbranch_scc1 Ls_je_%=\n"
  "s_cmp_lt_i32 s52, 5\n"
  "s_cbranch_scc1 Ls_w0_%=\n"
  "v_mfma_f32_16x16x32_bf16 v[48:51], v[40:43], v[92:95], 0\n"
  "v_mfma_f32_16x16x32_bf16 v[52:55], v[40:43], v[96:99], 0\n"
  "v_mfma_f32_16x16x32_bf16 v[56:59], v[40:43], v[100:103], 0\n"
  "v_mfma_f32_16x16x32_bf16 v[40:43], v[40:43], v[104:107], 0\n"
  "Ls_w0_%=:\n"
  "v_lshlrev_b64 v[80:81], 16, v[8:9]\n"
  "v_lshlrev_b64 v[82:83], 16, v[10:11]\n"
  "v_pk_fma_f32 v[72:73], v[8:9], v[60:61], v[72:73]\n"
  "v_pk_fma_f32 v[78:79], v[10:11], v[36:37], v[78:79]\n"
  "v_pk_fma_f32 v[74:75], v[80:81], v[64:65], v[74:75]\n"
  "v_pk_fma_f32 v[76:77], v[82:83], v[68:69], v[76:77]\n"
  "s_cmp_lt_i32 s52, 4\n"
  "s_cbranch_scc1 Ls_je_%=\n"
  "v_lshlrev_b64 v[80:81], 16, v[12:13]\n"
  "v_lshlrev_b64 v[82:83], 16, v[14:15]\n"
  "v_pk_fma_f32 v[72:73], v[12:13], v[62:63], v[72:73]\n"
  "v_pk_fma_f32 v[78:79], v[14:15], v[38:39], v[78:79]\n"
  "v_pk_fma_f32 v[74:75], v[80:81], v[66:67], v[74:75]\n"
  "v_pk_fma_f32 v[76:77], v[82:83], v[70:71], v[76:77]\n"
  "s_cmp_lt_i32 s52, 5\n"
  "s_cbranch_scc1 Ls_je_%=\n"
  "s_cmp_lt_i32 s52, 7\n"
  "s_cbranch_scc1 Ls_w1_%=\n"
  "v_mfma_f32_16x16x32_bf16 v[60:63], v[44:47], v[92:95], 0\n"
  "v_mfma_f32_16x16x32_bf16 v[64:67], v[44:47], v[96:99], 0\n"
  "v_mfma_f32_16x16x32_bf16 v[68:71], v[44:47], v[100:103], 0\n"
  "v_mfma_f32_16x16x32_bf16 v[44:47], v[44:47], v[104:107], 0\n"
  "Ls_w1_%=:\n"
  "v_lshlrev_b64 v[80:81], 16, v[16:17]\n"
  "v_lshlrev_b64 v[82:83], 16, v[18:19]\n"
  "v_pk_fma_f32 v[72:73], v[16:17], v[48:49], v[72:73]\n"
  "v_pk_fma_f32 v[78:79], v[18:19], v[40:41], v[78:79]\n"
  "v_pk_fma_f32 v[74:75], v[80:81], v[52:53], v[74:75]\n"
  "v_pk_fma_f32 v[76:77], v[82:83], v[56:57], v[76:77]\n"
  "s_cmp_lt_i32 s52, 6\n"
  "s_cbranch_scc1 Ls_je_%=\n"
  "v_lshlrev_b64 v[80:81], 16, v[20:21]\n"
  "v_lshlrev_b64 v[82:83], 16, v[22:23]\n"
  "v_pk_fma_f32 v[72:73], v[20:21], v[50:51], v[72:73]\n"
  "v_pk_fma_f32 v[78:79], v[22:23], v[42:43], v[78:79]\n"
  "v_pk_fma_f32 v[74:75], v[80:81], v[54:55], v[74:75]\n"
  "v_pk_fma_f32 v[76:77], v[82:83], v[58:59], v[76:77]\n"
  "s_cmp_lt_i32 s52, 7\n"
  "s_cbranch_scc1 Ls_je_%=\n"
  "v_lshlrev_b64 v[80:81], 16, v[24:25]\n"
  "v_lshlrev_b64 v[82:83], 16, v[26:27]\n"
  "v_pk_fma_f32 v[72:73], v[24:25], v[60:61], v[72:73]\n"
  "v_pk_fma_f32 v[78:79], v[26:27], v[44:45], v[78:79]\n"
  "v_pk_fma_f32 v[74:75], v[80:81], v[64:65], v[74:75]\n"
  "v_pk_fma_f32 v[76:77], v[82:83], v[68:69], v[76:77]\n"
  "s_cmp_lt_i32 s52, 8\n"
  "s_cbranch_scc1 Ls_je_%=\n"
  "v_lshlrev_b64 v[80:81], 16, v[28:29]\n"
  "v_lshlrev_b64 v[82:83], 16, v[30:31]\n"
  "v_pk_fma_f32 v[72:73], v[28:29], v[62:63], v[72:73]\n"
  "v_pk_fma_f32 v[78:79], v[30:31], v[46:47], v[78:79]\n"
  "v_pk_fma_f32 v[74:75], v[80:81], v[66:67], v[74:75]\n"
  "v_pk_fma_f32 v[76:77], v[82:83], v[70:71], v[76:77]\n"
  "Ls_je_%=:\n"
  "s_waitcnt lgkmcnt(0)\n"
  "s_cmp_lt_i32 s54, 8\n"
  "s_cbranch_scc1 Ls_gs_%=\n"
  "buffer_load_dwordx4 v[0:3], v84, s[40:43], 0 offen\n"
  "buffer_load_dwordx4 v[4:7], v85, s[40:43], 0 offen\n"
  "buffer_load_dwordx4 v[8:11], v86, s[40:43], 0 offen\n"
  "buffer_load_dwordx4 v[12:15], v87, s[40:43], 0 offen\n"
  "buffer_load_dwordx4 v[16:19], v88, s[40:43], 0 offen\n"
  "buffer_load_dwordx4 v[20:23], v89, s[40:43], 0 offen\n"
  "buffer_load_dwordx4 v[24:27], v90, s[40:43], 0 offen\n"
  "buffer_load_dwordx4 v[28:31], v91, s[40:43], 0 offen\n"
  "s_branch Ls_gl_%=\n"
  "Ls_gs_%=:\n"
  "s_cmp_lt_i32 s54, 1\n"
  "s_cbranch_scc1 Ls_gl_%=\n"
  "buffer_load_dwordx4 v[0:3], v84, s[40:43], 0 offen\n"
  "s_cmp_lt_i32 s54, 2\n"
  "s_cbranch_scc1 Ls_gl_%=\n"
  "buffer_load_dwordx4 v[4:7], v85, s[40:43], 0 offen\n"
  "s_cmp_lt_i32 s54, 3\n"
  "s_cbranch_scc1 Ls_gl_%=\n"
  "buffer_load_dwordx4 v[8:11], v86, s[40:43], 0 offen\n"
  "s_cmp_lt_i32 s54, 4\n"
  "s_cbranch_scc1 Ls_gl_%=\n"
  "buffer_load_dwordx4 v[12:15], v87, s[40:43], 0 offen\n"
  "s_cmp_lt_i32 s54, 5\n"
  "s_cbranch_scc1 Ls_gl_%=\n"
  "buffer_load_dwordx4 v[16:19], v88, s[40:43], 0 offen\n"
  "s_cmp_lt_i32 s54, 6\n"
  "s_cbranch_scc1 Ls_gl_%=\n"
  "buffer_load_dwordx4 v[20:23], v89, s[40:43], 0 offen\n"
  "s_cmp_lt_i32 s54, 7\n"
  "s_cbranch_scc1 Ls_gl_%=\n"
  "buffer_load_dwordx4 v[24:27], v90, s[40:43], 0 offen\n"
  "s_cmp_lt_i32 s54, 8\n"
  "s_cbranch_scc1 Ls_gl_%=\n"
  "buffer_load_dwordx4 v[28:31], v91, s[40:43], 0 offen\n"
  "Ls_gl_%=:\n"
  "s_cmp_eq_u32 s56, 0\n"
  "s_cbranch_scc1 Ls_nr_%=\n"
  "s_cmp_lt_i32 s54, 0\n"
  "s_cbranch_scc1 Ls_ns_%=\n"
  "v_add_u32 v113, v113, v114\n"
  "buffer_load_dwordx4 v[84:87], v113, s[44:47], 0 offen\n"
  "Ls_ns_%=:\n"
  "v_cvt_pk_bf16_f32 v72, v72, v73\n"
  "v_cvt_pk_bf16_f32 v73, v74, v75\n"
  "v_cvt_pk_bf16_f32 v74, v76, v77\n"
  "v_cvt_pk_bf16_f32 v75, v78, v79\n"
  "buffer_store_dwordx4 v[72:75], v112, s[48:51], 0 offen nt\n"
  "v_mov_b32 v112, v113\n"
  "s_cmp_lt_i32 s54, 0\n"
  "s_cbranch_scc1 Ls_rot_%=\n"
  "Ls_nr_%=:\n"
  "s_setprio 0\n"
  "v_add_u32 v108, s55, v117\n"
  "ds_read_u16 v80, v108 offset:6\n"
  "ds_read_u16 v81, v108 offset:22\n"
  "ds_read_u16 v82, v108 offset:38\n"
  "ds_read_u16 v83, v108 offset:54\n"
  "ds_read_b32 v124, v123\n"
  "s_waitcnt lgkmcnt(1)\n"
  "v_mad_u32_u24 v80, v80, v119, v120\n"
  "ds_read_b128 v[32:35], v80\n"
  "v_mad_u32_u24 v81, v81, v119, v120\n"
  "ds_read_b128 v[36:39], v81\n"
  "v_mad_u32_u24 v82, v82, v119, v120\n"
  "ds_read_b128 v[40:43], v82\n"
  "v_mad_u32_u24 v83, v83, v119, v120\n"
  "ds_read_b128 v[44:47], v83\n"
  "s_waitcnt lgkmcnt(3)\n"
  "v_mfma_f32_16x16x32_bf16 v[48:51], v[32:35], v[92:95], 0\n"
  "v_mfma_f32_16x16x32_bf16 v[52:55], v[32:35], v[96:99], 0\n"
  "v_mfma_f32_16x16x32_bf16 v[56:59], v[32:35], v[100:103], 0\n"
  "v_mfma_f32_16x16x32_bf16 v[32:35], v[32:35], v[104:107], 0\n"
  "s_cmp_lt_i32 s54, 3\n"
  "s_cbranch_scc1 Ls_rot_%=\n"
  "s_waitcnt lgkmcnt(2)\n"
  "v_mfma_f32_16x16x32_bf16 v[60:63], v[36:39], v[92:95], 0\n"
  "v_mfma_f32_16x16x32_bf16 v[64:67], v[36:39], v[96:99], 0\n"
  "v_mfma_f32_16x16x32_bf16 v[68:71], v[36:39], v[100:103], 0\n"
  "v_mfma_f32_16x16x32_bf16 v[36:39], v[36:39], v[104:107], 0\n"
  "Ls_rot_%=:\n"
  "s_mov_b32 s52, s54\n"
  "s_mov_b32 s53, s56\n"
  "s_cmp_ge_i32 s52, 0\n"
  "s_cbranch_scc1 Ls_top_%=\n"
  "s_waitcnt lgkmcnt(0)\n"
  "s_nop 7\n"
      : "+{v123}"(tick), "+{v108}"(t0)
      : "{s[40:43]}"(srdT), "{s[44:47]}"(srdS), "{s[48:51]}"(srdO),
        "{v114}"(oc), "{v117}"(lb), "{v118}"(ls), "{v119}"(amask), "{v120}"(aoff), "{v121}"(qaddr), "{v122}"(four)
      : "v0", "v1", "v2", "v3", "v4", "v5", "v6", "v7", "v8", "v9", "v10", "v11", "v12", "v13", "v14", "v15", "v16", "v17", "v18", "v19", "v20", "v21", "v22", "v23", "v24", "v25", "v26", "v27", "v28", "v29", "v30", "v31", "v32", "v33", "v34", "v35", "v36", "v37", "v38", "v39", "v40", "v41", "v42", "v43", "v44", "v45", "v46", "v47", "v48", "v49", "v50", "v51", "v52", "v53", "v54", "v55", "v56", "v57", "v58", "v59", "v60", "v61", "v62", "v63", "v64", "v65", "v66", "v67", "v68", "v69", "v70", "v71", "v72", "v73", "v74", "v75", "v76", "v77", "v78", "v79", "v80", "v81", "v82", "v83", "v84", "v85", "v86", "v87", "v88", "v89", "v90", "v91", "v92", "v93", "v94", "v95", "v96", "v97", "v98", "v99", "v100", "v101", "v102", "v103", "v104", "v105", "v106", "v107", "v112", "v113", "v124", SCLOB);
}
__device__ __forceinline__ void phase_vec(u32x4 srdT, u32x4 srdS, u32x4 srdO) {
  const int lane = lane_id(), p = lane & 15, g = lane >> 4;
  u32 oc = 4 * p;
  u32 m01 = ((p & 3) == g) ? 1u : 0u;
  u32 la = m01 ? (L_REC + (8 * g + (p >> 2)) * 8) : L_LUT, lb = L_REC + (8 * (p >> 2) + ((p >> 1) & 1)) * 8, ls = L_SRC + 32 * g;
  u32 amask = ((g >> 1) == (p & 1)) ? 32u : 0u, aoff = L_LUT + 16 * (g & 1) - amask * LUT_BASE_BITS;
  u32 qaddr = L_MISC + 12, four = 4, tick = 0, t1 = L_WV + lane * 16;
  asm volatile(
  "s_mov_b64 exec, 1\n"
  "ds_add_rtn_u32 v123, v121, v122\n"
  "s_mov_b64 exec, -1\n"
  "ds_read_b128 v[104:107], v75\n"
  "ds_read_b128 v[108:111], v75 offset:1024\n"
  "v_mov_b32 v76, 0\n"
  "v_mov_b32 v77, 0\n"
  "v_mov_b32 v78, 0\n"
  "v_mov_b32 v79, 0\n"
  "v_mov_b32 v80, 0\n"
  "v_mov_b32 v81, 0\n"
  "v_mov_b32 v82, 0\n"
  "v_mov_b32 v83, 0\n"
  "s_waitcnt lgkmcnt(0)\n"
  "ds_read_b32 v124, v123\n"
  "s_waitcnt lgkmcnt(0)\n"
  "v_readfirstlane_b32 s57, v124\n"
  "s_nop 0\n"
  "s_ashr_i32 s54, s57, 18\n"
  "s_and_b32 s58, s57, 0x3ff\n"
  "s_bfe_u32 s57, s57, 0x8000a\n"
  "s_add_i32 s58, s58, -74432\n"
  "s_lshl_b32 s55, s57, 8\n"
  "v_add_u32 v74, s58, v118\n"
  "s_mov_b32 s56, 1\n"
  "ds_read_b32 v113, v74\n"
  "s_cmp_lt_i32 s54, 0\n"
  "s_cbranch_scc1 Lv_rot_%=\n"
  "s_cmp_gt_i32 s54, 8\n"
  "s_cbranch_scc1 Lv_ntp_%=\n"
  "s_mov_b64 exec, 1\n"
  "ds_add_rtn_u32 v123, v121, v122\n"
  "s_mov_b64 exec, -1\n"
  "Lv_ntp_%=:\n"
  "s_lshr_b32 s57, s55, 1\n"
  "v_add_u32 v74, s57, v118\n"
  "ds_read_b128 v[88:91], v74\n"
  "ds_read_b128 v[92:95], v74 offset:16\n"
  "s_waitcnt lgkmcnt(0)\n"
  "s_cmp_lt_i32 s54, 1\n"
  "s_cbranch_scc1 Lv_gp_%=\n"
  "buffer_load_dwordx4 v[0:3], v88, s[40:43], 0 offen\n"
  "s_cmp_lt_i32 s54, 2\n"
  "s_cbranch_scc1 Lv_gp_%=\n"
  "buffer_load_dwordx4 v[4:7], v89, s[40:43], 0 offen\n"
  "s_cmp_lt_i32 s54, 3\n"
  "s_cbranch_scc1 Lv_gp_%=\n"
  "buffer_load_dwordx4 v[8:11], v90, s[40:43], 0 offen\n"
  "s_cmp_lt_i32 s54, 4\n"
  "s_cbranch_scc1 Lv_gp_%=\n"
  "buffer_load_dwordx4 v[12:15], v91, s[40:43], 0 offen\n"
  "s_cmp_lt_i32 s54, 5\n"
  "s_cbranch_scc1 Lv_gp_%=\n"
  "buffer_load_dwordx4 v[16:19], v92, s[40:43], 0 offen\n"
  "s_cmp_lt_i32 s54, 6\n"
  "s_cbranch_scc1 Lv_gp_%=\n"
  "buffer_load_dwordx4 v[20:23], v93, s[40:43], 0 offen\n"
  "s_cmp_lt_i32 s54, 7\n"
  "s_cbranch_scc1 Lv_gp_%=\n"
  "buffer_load_dwordx4 v[24:27], v94, s[40:43], 0 offen\n"
  "s_cmp_lt_i32 s54, 8\n"
  "s_cbranch_scc1 Lv_gp_%=\n"
  "buffer_load_dwordx4 v[28:31], v95, s[40:43], 0 offen\n"
  "Lv_gp_%=:\n"
  "v_add_u32 v113, v113, v114\n"
  "buffer_load_dword v88, v113, s[44:47], 0 offen offset:256\n"
  "buffer_load_dword v89, v113, s[44:47], 0 offen offset:512\n"
  "buffer_load_dword v90, v113, s[44:47], 0 offen offset:768\n"
  "v_add_u32 v74, s55, v117\n"
  "ds_read_u16 v70, v74 offset:6\n"
  "ds_read_u16 v71, v74 offset:22\n"
  "ds_read_u16 v72, v74 offset:38\n"
  "ds_read_u16 v73, v74 offset:54\n"
  "ds_read_b32 v124, v123\n"
  "v_mad_u32_u24 v75, s55, v116, v115\n"
  "v_mov_b32 v112, v113\n"
  "s_waitcnt lgkmcnt(1)\n"
  "v_mad_u32_u24 v70, v70, v119, v120\n"
  "ds_read_b128 v[48:51], v70\n"
  "v_mad_u32_u24 v71, v71, v119, v120\n"
  "ds_read_b128 v[52:55], v71\n"
  "v_mad_u32_u24 v72, v72, v119, v120\n"
  "ds_read_b128 v[56:59], v72\n"
  "v_mad_u32_u24 v73, v73, v119, v120\n"
  "ds_read_b128 v[60:63], v73\n"
  "ds_read_b64_tr_b16 v[84:85], v75\n"
  "ds_read_b64_tr_b16 v[86:87], v75 offset:32\n"
  "s_waitcnt lgkmcnt(5)\n"
  "v_mfma_f32_16x16x32_bf16 v[32:35], v[48:51], v[104:107], 0\n"
  "v_mfma_f32_16x16x32_bf16 v[48:51], v[48:51], v[108:111], 0\n"
  "s_waitcnt lgkmcnt(4)\n"
  "v_mfma_f32_16x16x32_bf16 v[36:39], v[52:55], v[104:107], 0\n"
  "v_mfma_f32_16x16x32_bf16 v[52:55], v[52:55], v[108:111], 0\n"
  "s_waitcnt lgkmcnt(3)\n"
  "v_mfma_f32_16x16x32_bf16 v[40:43], v[56:59], v[104:107], 0\n"
  "v_mfma_f32_16x16x32_bf16 v[56:59], v[56:59], v[108:111], 0\n"
  "s_waitcnt lgkmcnt(2)\n"
  "v_mfma_f32_16x16x32_bf16 v[44:47], v[60:63], v[104:107], 0\n"
  "v_mfma_f32_16x16x32_bf16 v[60:63], v[60:63], v[108:111], 0\n"
  "s_waitcnt vmcnt(0)\n"
  "s_branch Lv_rot_%=\n"
  "Lv_cont_%=:\n"
  "s_sub_i32 s54, s52, 8\n"
  "s_add_i32 s55, s55, 256\n"
  "s_mov_b32 s56, 0\n"
  "s_branch Lv_join_%=\n"
  "Lv_top_%=:\n"
  "s_waitcnt lgkmcnt(0)\n"
  "s_cmp_gt_i32 s52, 8\n"
  "s_cbranch_scc1 Lv_cont_%=\n"
  "v_readfirstlane_b32 s57, v124\n"
  "s_nop 0\n"
  "s_ashr_i32 s54, s57, 18\n"
  "s_and_b32 s58, s57, 0x3ff\n"
  "s_bfe_u32 s57, s57, 0x8000a\n"
  "s_add_i32 s58, s58, -74432\n"
  "s_lshl_b32 s55, s57, 8\n"
  "v_add_u32 v74, s58, v118\n"
  "s_mov_b32 s56, 1\n"
  "ds_read_b32 v113, v74\n"
  "Lv_join_%=:\n"
  "s_cmp_gt_i32 s54, 8\n"
  "s_cbranch_scc1 Lv_ntl_%=\n"
  "s_mov_b64 exec, 1\n"
  "ds_add_rtn_u32 v123, v121, v122\n"
  "s_mov_b64 exec, -1\n"
  "Lv_ntl_%=:\n"
  "s_setprio 2\n"
  "s_cmp_eq_u32 s53, 0\n"
  "s_cbranch_scc1 Lv_nf_%=\n"
  "s_waitcnt vmcnt(3)\n"
  "v_lshlrev_b32 v64, 16, v88\n"
  "v_and_b32 v65, 0xffff0000, v88\n"
  "v_lshlrev_b32 v66, 16, v89\n"
  "v_and_b32 v67, 0xffff0000, v89\n"
  "v_lshlrev_b32 v68, 16, v90\n"
  "v_and_b32 v69, 0xffff0000, v90\n"
  "s_lshr_b32 s57, s55, 1\n"
  "v_add_u32 v74, s57, v118\n"
  "ds_read_b128 v[88:91], v74\n"
  "ds_read_b128 v[92:95], v74 offset:16\n"
  "s_cmp_lt_i32 s52, 1\n"
  "s_cbranch_scc1 Lv_je_%=\n"
  "s_branch Lv_j0_%=\n"
  "Lv_nf_%=:\n"
  "s_lshr_b32 s57, s55, 1\n"
  "v_add_u32 v74, s57, v118\n"
  "ds_read_b128 v[88:91], v74\n"
  "ds_read_b128 v[92:95], v74 offset:16\n"
  "s_waitcnt vmcnt(0)\n"
  "Lv_j0_%=:\n"
  "v_lshlrev_b64 v[70:71], 16, v[0:1]\n"
  "v_lshlrev_b64 v[72:73], 16, v[2:3]\n"
  "v_pk_mul_f32 v[48:49], v[2:3], v[48:49]\n"
  "v_pk_fma_f32 v[64:65], v[0:1], v[32:33], v[64:65]\n"
  "v_pk_fma_f32 v[66:67], v[70:71], v[32:33], v[66:67]\n"
  "v_pk_fma_f32 v[68:69], v[72:73], v[32:33], v[68:69]\n"
  "s_cmp_lt_i32 s52, 2\n"
  "s_cbranch_scc1 Lv_c0_%=\n"
  "v_lshlrev_b64 v[70:71], 16, v[4:5]\n"
  "v_lshlrev_b64 v[72:73], 16, v[6:7]\n"
  "v_pk_mul_f32 v[50:51], v[6:7], v[50:51]\n"
  "v_pk_fma_f32 v[64:65], v[4:5], v[34:35], v[64:65]\n"
  "v_pk_fma_f32 v[66:67], v[70:71], v[34:35], v[66:67]\n"
  "v_pk_fma_f32 v[68:69], v[72:73], v[34:35], v[68:69]\n"
  "Lv_c0_%=:\n"
  "v_cvt_pk_bf16_f32 v76, v48, v50\n"
  "v_cvt_pk_bf16_f32 v80, v49, v51\n"
  "s_cmp_lt_i32 s52, 3\n"
  "s_cbranch_scc1 Lv_je_%=\n"
  "v_lshlrev_b64 v[70:71], 16, v[8:9]\n"
  "v_lshlrev_b64 v[72:73], 16, v[10:11]\n"
  "v_pk_mul_f32 v[52:53], v[10:11], v[52:53]\n"
  "v_pk_fma_f32 v[64:65], v[8:9], v[36:37], v[64:65]\n"
  "v_pk_fma_f32 v[66:67], v[70:71], v[36:37], v[66:67]\n"
  "v_pk_fma_f32 v[68:69], v[72:73], v[36:37], v[68:69]\n"
  "s_cmp_lt_i32 s52, 4\n"
  "s_cbranch_scc1 Lv_c1_%=\n"
  "v_lshlrev_b64 v[70:71], 16, v[12:13]\n"
  "v_lshlrev_b64 v[72:73], 16, v[14:15]\n"
  "v_pk_mul_f32 v[54:55], v[14:15], v[54:55]\n"
  "v_pk_fma_f32 v[64:65], v[12:13], v[38:39], v[64:65]\n"
  "v_pk_fma_f32 v[66:67], v[70:71], v[38:39], v[66:67]\n"
  "v_pk_fma_f32 v[68:69], v[72:73], v[38:39], v[68:69]\n"
  "Lv_c1_%=:\n"
  "v_cvt_pk_bf16_f32 v77, v52, v54\n"
  "v_cvt_pk_bf16_f32 v81, v53, v55\n"
  "s_cmp_lt_i32 s52, 5\n"
  "s_cbranch_scc1 Lv_je_%=\n"
  "v_lshlrev_b64 v[70:71], 16, v[16:17]\n"
  "v_lshlrev_b64 v[72:73], 16, v[18:19]\n"
  "v_pk_mul_f32 v[56:57], v[18:19], v[56:57]\n"
  "v_pk_fma_f32 v[64:65], v[16:17], v[40:41], v[64:65]\n"
  "v_pk_fma_f32 v[66:67], v[70:71], v[40:41], v[66:67]\n"
  "v_pk_fma_f32 v[68:69], v[72:73], v[40:41], v[68:69]\n"
  "s_cmp_lt_i32 s52, 6\n"
  "s_cbranch_scc1 Lv_c2_%=\n"
  "v_lshlrev_b64 v[70:71], 16, v[20:21]\n"
  "v_lshlrev_b64 v[72:73], 16, v[22:23]\n"
  "v_pk_mul_f32 v[58:59], v[22:23], v[58:59]\n"
  "v_pk_fma_f32 v[64:65], v[20:21], v[42:43], v[64:65]\n"
  "v_pk_fma_f32 v[66:67], v[70:71], v[42:43], v[66:67]\n"
  "v_pk_fma_f32 v[68:69], v[72:73], v[42:43], v[68:69]\n"
  "Lv_c2_%=:\n"
  "v_cvt_pk_bf16_f32 v78, v56, v58\n"
  "v_cvt_pk_bf16_f32 v82, v57, v59\n"
  "s_cmp_lt_i32 s52, 7\n"
  "s_cbranch_scc1 Lv_je_%=\n"
  "v_lshlrev_b64 v[70:71], 16, v[24:25]\n"
  "v_lshlrev_b64 v[72:73], 16, v[26:27]\n"
  "v_pk_mul_f32 v[60:61], v[26:27], v[60:61]\n"
  "v_pk_fma_f32 v[64:65], v[24:25], v[44:45], v[64:65]\n"
  "v_pk_fma_f32 v[66:67], v[70:71], v[44:45], v[66:67]\n"
  "v_pk_fma_f32 v[68:69], v[72:73], v[44:45], v[68:69]\n"
  "s_cmp_lt_i32 s52, 8\n"
  "s_cbranch_scc1 Lv_c3_%=\n"
  "v_lshlrev_b64 v[70:71], 16, v[28:29]\n"
  "v_lshlrev_b64 v[72:73], 16, v[30:31]\n"
  "v_pk_mul_f32 v[62:63], v[30:31], v[62:63]\n"
  "v_pk_fma_f32 v[64:65], v[28:29], v[46:47], v[64:65]\n"
  "v_pk_fma_f32 v[66:67], v[70:71], v[46:47], v[66:67]\n"
  "v_pk_fma_f32 v[68:69], v[72:73], v[46:47], v[68:69]\n"
  "Lv_c3_%=:\n"
  "v_cvt_pk_bf16_f32 v79, v60, v62\n"
  "v_cvt_pk_bf16_f32 v83, v61, v63\n"
  "Lv_je_%=:\n"
  "s_waitcnt lgkmcnt(0)\n"
  "s_cmp_lt_i32 s54, 8\n"
  "s_cbranch_scc1 Lv_gs_%=\n"
  "buffer_load_dwordx4 v[0:3], v88, s[40:43], 0 offen\n"
  "buffer_load_dwordx4 v[4:7], v89, s[40:43], 0 offen\n"
  "buffer_load_dwordx4 v[8:11], v90, s[40:43], 0 offen\n"
  "buffer_load_dwordx4 v[12:15], v91, s[40:43], 0 offen\n"
  "buffer_load_dwordx4 v[16:19], v92, s[40:43], 0 offen\n"
  "buffer_load_dwordx4 v[20:23], v93, s[40:43], 0 offen\n"
  "buffer_load_dwordx4 v[24:27], v94, s[40:43], 0 offen\n"
  "buffer_load_dwordx4 v[28:31], v95, s[40:43], 0 offen\n"
  "s_branch Lv_gl_%=\n"
  "Lv_gs_%=:\n"
  "s_cmp_lt_i32 s54, 1\n"
  "s_cbranch_scc1 Lv_gl_%=\n"
  "buffer_load_dwordx4 v[0:3], v88, s[40:43], 0 offen\n"
  "s_cmp_lt_i32 s54, 2\n"
  "s_cbranch_scc1 Lv_gl_%=\n"
  "buffer_load_dwordx4 v[4:7], v89, s[40:43], 0 offen\n"
  "s_cmp_lt_i32 s54, 3\n"
  "s_cbranch_scc1 Lv_gl_%=\n"
  "buffer_load_dwordx4 v[8:11], v90, s[40:43], 0 offen\n"
  "s_cmp_lt_i32 s54, 4\n"
  "s_cbranch_scc1 Lv_gl_%=\n"
  "buffer_load_dwordx4 v[12:15], v91, s[40:43], 0 offen\n"
  "s_cmp_lt_i32 s54, 5\n"
  "s_cbranch_scc1 Lv_gl_%=\n"
  "buffer_load_dwordx4 v[16:19], v92, s[40:43], 0 offen\n"
  "s_cmp_lt_i32 s54, 6\n"
  "s_cbranch_scc1 Lv_gl_%=\n"
  "buffer_load_dwordx4 v[20:23], v93, s[40:43], 0 offen\n"
  "s_cmp_lt_i32 s54, 7\n"
  "s_cbranch_scc1 Lv_gl_%=\n"
  "buffer_load_dwordx4 v[24:27], v94, s[40:43], 0 offen\n"
  "s_cmp_lt_i32 s54, 8\n"
  "s_cbranch_scc1 Lv_gl_%=\n"
  "buffer_load_dwordx4 v[28:31], v95, s[40:43], 0 offen\n"
  "Lv_gl_%=:\n"
  "s_cmp_eq_u32 s56, 0\n"
  "s_cbranch_scc1 Lv_nr_%=\n"
  "s_cmp_lt_i32 s54, 0\n"
  "s_cbranch_scc1 Lv_nr_%=\n"
  "v_add_u32 v113, v113, v114\n"
  "buffer_load_dword v88, v113, s[44:47], 0 offen offset:256\n"
  "buffer_load_dword v89, v113, s[44:47], 0 offen offset:512\n"
  "buffer_load_dword v90, v113, s[44:47], 0 offen offset:768\n"
  "Lv_nr_%=:\n"
  "s_setprio 0\n"
  "v_add_u32 v74, s55, v117\n"
  "ds_read_u16 v70, v74 offset:6\n"
  "ds_read_u16 v71, v74 offset:22\n"
  "ds_read_u16 v72, v74 offset:38\n"
  "ds_read_u16 v73, v74 offset:54\n"
  "ds_read_b32 v124, v123\n"
  "s_cmp_eq_u32 s53, 0\n"
  "s_cbranch_scc1 Lv_ua_%=\n"
  "v_mfma_f32_16x16x32_bf16 v[96:99], v[84:87], v[76:79], 0\n"
  "v_mfma_f32_16x16x32_bf16 v[100:103], v[84:87], v[80:83], 0\n"
  "s_branch Lv_ub_%=\n"
  "Lv_ua_%=:\n"
  "v_mfma_f32_16x16x32_bf16 v[96:99], v[84:87], v[76:79], v[96:99]\n"
  "v_mfma_f32_16x16x32_bf16 v[100:103], v[84:87], v[80:83], v[100:103]\n"
  "Lv_ub_%=:\n"
  "v_mad_u32_u24 v75, s55, v116, v115\n"
  "s_waitcnt lgkmcnt(1)\n"
  "ds_read_b64_tr_b16 v[84:85], v75\n"
  "ds_read_b64_tr_b16 v[86:87], v75 offset:32\n"
  "v_mad_u32_u24 v70, v70, v119, v120\n"
  "ds_read_b128 v[48:51], v70\n"
  "v_mad_u32_u24 v71, v71, v119, v120\n"
  "ds_read_b128 v[52:55], v71\n"
  "s_cmp_lt_i32 s54, 5\n"
  "s_cbranch_scc1 Lv_ae_%=\n"
  "v_mad_u32_u24 v72, v72, v119, v120\n"
  "ds_read_b128 v[56:59], v72\n"
  "s_cmp_lt_i32 s54, 7\n"
  "s_cbranch_scc1 Lv_ae_%=\n"
  "v_mad_u32_u24 v73, v73, v119, v120\n"
  "ds_read_b128 v[60:63], v73\n"
  "Lv_ae_%=:\n"
  "s_cmp_eq_u32 s56, 0\n"
  "s_cbranch_scc1 Lv_no_%=\n"
  "v_add_f32 v64, v64, v96\n"
  "v_add_f32 v65, v65, v100\n"
  "v_add_f32 v66, v66, v97\n"
  "v_add_f32 v67, v67, v101\n"
  "v_add_f32 v68, v68, v98\n"
  "v_add_f32 v69, v69, v102\n"
  "v_cvt_pk_bf16_f32 v64, v64, v65\n"
  "buffer_store_dword v64, v112, s[48:51], 0 offen offset:256 nt\n"
  "v_cvt_pk_bf16_f32 v66, v66, v67\n"
  "buffer_store_dword v66, v112, s[48:51], 0 offen offset:512 nt\n"
  "v_cvt_pk_bf16_f32 v68, v68, v69\n"
  "buffer_store_dword v68, v112, s[48:51], 0 offen offset:768 nt\n"
  "v_mov_b32 v112, v113\n"
  "Lv_no_%=:\n"
  "s_waitcnt lgkmcnt(0)\n"
  "v_mfma_f32_16x16x32_bf16 v[32:35], v[48:51], v[104:107], 0\n"
  "v_mfma_f32_16x16x32_bf16 v[48:51], v[48:51], v[108:111], 0\n"
  "s_cmp_lt_i32 s54, 3\n"
  "s_cbranch_scc1 Lv_rot_%=\n"
  "v_mfma_f32_16x16x32_bf16 v[36:39], v[52:55], v[104:107], 0\n"
  "v_mfma_f32_16x16x32_bf16 v[52:55], v[52:55], v[108:111], 0\n"
  "s_cmp_lt_i32 s54, 5\n"
  "s_cbranch_scc1 Lv_rot_%=\n"
  "v_mfma_f32_16x16x32_bf16 v[40:43], v[56:59], v[104:107], 0\n"
  "v_mfma_f32_16x16x32_bf16 v[56:59], v[56:59], v[108:111], 0\n"
  "s_cmp_lt_i32 s54, 7\n"
  "s_cbranch_scc1 Lv_rot_%=\n"
  "v_mfma_f32_16x16x32_bf16 v[44:47], v[60:63], v[104:107], 0\n"
  "v_mfma_f32_16x16x32_bf16 v[60:63], v[60:63], v[108:111], 0\n"
  "Lv_rot_%=:\n"
  "s_mov_b32 s52, s54\n"
  "s_mov_b32 s53, s56\n"
  "s_cmp_ge_i32 s52, 0\n"
  "s_cbranch_scc1 Lv_top_%=\n"
  "s_waitcnt lgkmcnt(0)\n"
  "s_nop 7\n"
      : "+{v123}"(tick), "+{v75}"(t1)
      : "{s[40:43]}"(srdT), "{s[44:47]}"(srdS), "{s[48:51]}"(srdO),
        "{v114}"(oc), "{v115}"(la), "{v116}"(m01), "{v117}"(lb), "{v118}"(ls), "{v119}"(amask), "{v120}"(aoff), "{v121}"(qaddr), "{v122}"(four)
      : "v0", "v1", "v2", "v3", "v4", "v5", "v6", "v7", "v8", "v9", "v10", "v11", "v12", "v13", "v14", "v15", "v16", "v17", "v18", "v19", "v20", "v21", "v22", "v23", "v24", "v25", "v26", "v27", "v28", "v29", "v30", "v31", "v32", "v33", "v34", "v35", "v36", "v37", "v38", "v39", "v40", "v41", "v42", "v43", "v44", "v45", "v46", "v47", "v48", "v49", "v50", "v51", "v52", "v53", "v54", "v55", "v56", "v57", "v58", "v59", "v60", "v61", "v62", "v63", "v64", "v65", "v66", "v67", "v68", "v69", "v70", "v71", "v72", "v73", "v74", "v76", "v77", "v78", "v79", "v80", "v81", "v82", "v83", "v84", "v85", "v86", "v87", "v88", "v89", "v90", "v91", "v92", "v93", "v94", "v95", "v96", "v97", "v98", "v99", "v100", "v101", "v102", "v103", "v104", "v105", "v106", "v107", "v108", "v109", "v110", "v111", "v112", "v113", "v124", SCLOB);
}

__device__ __forceinline__ u32 rankoff(u32 rank) { return ((rank >> 5) << 8) | ((rank & 28) << 1); }
__device__ __forceinline__ void place(u32 r0, u32 r1, u32 sv, u32 a) {
  *(LAS u32x2*)a = u32x2{r0, r1};
  st32((a >> 1) + (L_SRC - L_REC / 2), sv + ((~a & 0xc0u) << 2));
}
extern "C" __global__ __launch_bounds__(1024) void k_main(const u16* __restrict__ sv, u16* __restrict__ out, const char* __restrict__ wfrag,
                                               const char* __restrict__ lut, int N) {
  const char* tab = lut + LUT_BYTES;
  const u32* offs = (const u32*)(wfrag + W_OFFS);
  const char* recs = wfrag + W_RECS;
  const u32x4* ovf = (const u32x4*)(wfrag + W_OVF);
  asm volatile("" :: "s"(sv), "s"(out), "s"(wfrag), "s"(lut), "s"(N));
  const int t = threadIdx.x, lane = t & 63, w = __builtin_amdgcn_readfirstlane(t >> 6);
  const int xcd = blockIdx.x & 7, u = xcd >> 1, bin = (xcd & 1) * 32 + (blockIdx.x >> 3);
  const int n0 = bin * BIN, nn = __builtin_amdgcn_readfirstlane(max(0, min(BIN, N - n0)));
  auto piece = [&](int j) {
    const int i = j > 0 ? j + COLDP : -j;
    if (i < 63) { if (i < 62 || lane < 32) dma16(mksrd(lut, 0, LUT_BYTES, 0x00020000u), i * 1024 + lane * 16, L_LUT + i * 1024); }
    else if (i < 69) dma16(mksrd(wfrag, 0, 12288, 0x00020000u), (i < 67 ? 8192 + (i - 63) * 1024 : u * 2048 + (i - 67) * 1024) + lane * 16, L_WS + (i - 63) * 1024);
  };
  auto prefetch = [&](int j) {
    const u32x4 srdP = mksrd(tab + (size_t)4 * N * 256, 0, N * 256, 0x00020000u);
    u32 keep;
    asm volatile("s_mov_b32 %0, m0\n\ts_mov_b32 m0, %3\n\ts_nop 0\n\tbuffer_load_dword %1, %2, 0 offen lds\n\ts_mov_b32 m0, %0"
                 : "=&s"(keep) : "v"((((blockIdx.x >> 3) * 13 + j) * 64 + lane) * 128), "s"(srdP), "s"(L_DUM) : "memory");
  };
  auto clear = [&]() {
    u32 z = (u32)LUT_BASE_BITS << 16, a = L_SRC + t * 16; OPQ(a);
    *(LAS u32x4*)a = u32x4{0, 0, 0, 0};
    if (t < (L_REC - L_SRC - 16384) / 16) *(LAS u32x4*)(a + 16384) = u32x4{0, 0, 0, 0};
    a += L_REC - L_SRC; OPQ(a);
#pragma unroll
    for (int i = 0; i < 3; i++) *(LAS u32x4*)(a + i * 16384) = u32x4{0, z, 0, z};
    if (t < (L_END - L_REC - 49152) / 16) *(LAS u32x4*)(a + 49152) = u32x4{0, z, 0, z};
  };
  u32 nch, novr;
  const u32 smax = (u32)(N - 1) << 8;
  if (w == 15) {
    *(LAS u32x4*)(L_CNT + lane * 16) = u32x4{0, 0, 0, 0};
    if (lane < 20) *(LAS u32x4*)(L_HIST + lane * 16) = u32x4{0, 0, 0, 0};
    *(LAS u32x4*)(L_SORT + lane * 16) = u32x4{1020, 1020, 1020, 1020};
    clear();
    BAR();
#pragma unroll
    for (int i = 0; i < NE; i++) piece(i);
    __builtin_amdgcn_s_setprio(3);
    const int q0 = u * QN, nq0 = max(0, min(QN, nn - q0)), a0 = (nq0 + 3) & ~3, nq = (a0 + (nn - nq0) + 3) >> 2;
    u32x4 inc, sh, nid, s4;
    unsigned long long vm[4];
#pragma unroll
    for (int i = 0; i < 4; i++) {
      int n = lane * 4 + i;
      sh[i] = ((u32)(n - q0) < (u32)QN) ? 0u : 16u;
      inc[i] = 4u << sh[i]; nid[i] = n * 4;
      vm[i] = __builtin_amdgcn_ballot_w64(n < nn);
    }
    u32 mk, base, incl;
    u32 qoff = (lane >> 3) * 128 + (lane & 7) * 4, qm = lane < nq ? ~0u : 0u, sm = lane * 4 < a0 ? ~0u : 0u;
    OPQ(qoff); OPQ(qm); OPQ(sm);
    if (lane < 24) st32(L_ITV + 256 + lane * 4, 0xFFFC0000u);
    if (lane == 63) *(LAS u32x2*)(L_MISC + 8) = u32x2{L_ITS + 64, L_ITV};
    asm volatile("s_barrier\n"
  "ds_read_b128 v[80:83], v76 offset:0\n"
  "s_waitcnt lgkmcnt(0)\n"
  "v_min_u32 v84, 0xfc, v80\n"
  "v_min_u32 v85, 0xfc, v81\n"
  "v_min_u32 v86, 0xfc, v82\n"
  "v_min_u32 v87, 0xfc, v83\n"
  "s_mov_b64 exec, s[70:71]\n"
  "ds_add_rtn_u32 v64, v84, v64 offset:1024\n"
  "s_mov_b64 exec, s[72:73]\n"
  "ds_add_rtn_u32 v65, v85, v65 offset:1024\n"
  "s_mov_b64 exec, s[74:75]\n"
  "ds_add_rtn_u32 v66, v86, v66 offset:1024\n"
  "s_mov_b64 exec, s[76:77]\n"
  "ds_add_rtn_u32 v67, v87, v67 offset:1024\n"
  "s_mov_b64 exec, -1\n"
  "ds_read_b32 v88, v77 offset:1024\n"
  "v_lshl_or_b32 v80, v80, 10, v72\n"
  "v_lshl_or_b32 v81, v81, 10, v73\n"
  "v_lshl_or_b32 v82, v82, 10, v74\n"
  "v_lshl_or_b32 v83, v83, 10, v75\n"
  "s_waitcnt lgkmcnt(0)\n"
  "v_add_u32_dpp v88, v88, v88 row_shr:1 row_mask:0xf bank_mask:0xf bound_ctrl:1\n"
  "s_nop 1\n"
  "v_add_u32_dpp v88, v88, v88 row_shr:2 row_mask:0xf bank_mask:0xf bound_ctrl:1\n"
  "s_nop 1\n"
  "v_add_u32_dpp v88, v88, v88 row_shr:4 row_mask:0xf bank_mask:0xf bound_ctrl:1\n"
  "s_nop 1\n"
  "v_add_u32_dpp v88, v88, v88 row_shr:8 row_mask:0xf bank_mask:0xf bound_ctrl:1\n"
  "s_nop 1\n"
  "v_add_u32_dpp v88, v88, v88 row_bcast:15 row_mask:0xa bank_mask:0xf\n"
  "s_nop 1\n"
  "v_add_u32_dpp v88, v88, v88 row_bcast:31 row_mask:0xc bank_mask:0xf\n"
  "v_sub_u32 v88, v78, v88\n"
  "ds_bpermute_b32 v84, v84, v88\n"
  "ds_bpermute_b32 v85, v85, v88\n"
  "ds_bpermute_b32 v86, v86, v88\n"
  "ds_bpermute_b32 v87, v87, v88\n"
  "s_waitcnt lgkmcnt(3)\n"
  "v_add_u32 v84, v84, v64\n"
  "v_bfe_u32 v84, v84, v68, 16\n"
  "s_mov_b64 exec, s[70:71]\n"
  "ds_write_b32 v84, v80 offset:1344\n"
  "s_mov_b64 exec, -1\n"
  "s_waitcnt lgkmcnt(3)\n"
  "v_add_u32 v85, v85, v65\n"
  "v_bfe_u32 v85, v85, v69, 16\n"
  "s_mov_b64 exec, s[72:73]\n"
  "ds_write_b32 v85, v81 offset:1344\n"
  "s_mov_b64 exec, -1\n"
  "s_waitcnt lgkmcnt(3)\n"
  "v_add_u32 v86, v86, v66\n"
  "v_bfe_u32 v86, v86, v70, 16\n"
  "s_mov_b64 exec, s[74:75]\n"
  "ds_write_b32 v86, v82 offset:1344\n"
  "s_mov_b64 exec, -1\n"
  "s_waitcnt lgkmcnt(3)\n"
  "v_add_u32 v87, v87, v67\n"
  "v_bfe_u32 v87, v87, v71, 16\n"
  "s_mov_b64 exec, s[76:77]\n"
  "ds_write_b32 v87, v83 offset:1344\n"
  "s_mov_b64 exec, -1\n"
  "ds_read_b128 v[84:87], v76 offset:1344\n"
  "s_waitcnt lgkmcnt(0)\n"
  "v_max_u32 v88, v84, v85\n"
  "v_max3_u32 v88, v88, v86, v87\n"
  "v_add_u32 v89, 0x7000, v88\n"
  "v_lshrrev_b32 v89, 15, v89\n"
  "v_max_u32 v89, 1, v89\n"
  "v_and_b32 v89, v89, v79\n"
  "v_and_b32 v80, 0x3ff, v84\n"
  "v_and_b32 v81, 0x3ff, v85\n"
  "v_add_u32_dpp v90, v89, v89 row_shr:1 row_mask:0xf bank_mask:0xf bound_ctrl:1\n"
  "s_nop 1\n"
  "v_add_u32_dpp v90, v90, v90 row_shr:2 row_mask:0xf bank_mask:0xf bound_ctrl:1\n"
  "s_nop 1\n"
  "v_add_u32_dpp v90, v90, v90 row_shr:4 row_mask:0xf bank_mask:0xf bound_ctrl:1\n"
  "s_nop 1\n"
  "v_add_u32_dpp v90, v90, v90 row_shr:8 row_mask:0xf bank_mask:0xf bound_ctrl:1\n"
  "s_nop 1\n"
  "v_add_u32_dpp v90, v90, v90 row_bcast:15 row_mask:0xa bank_mask:0xf\n"
  "s_nop 1\n"
  "v_add_u32_dpp v90, v90, v90 row_bcast:31 row_mask:0xc bank_mask:0xf\n"
  "v_sub_u32 v89, v90, v89\n"
  "v_lshlrev_b32 v91, 8, v89\n"
  "v_and_b32 v82, 0x3ff, v86\n"
  "v_and_b32 v83, 0x3ff, v87\n"
  "v_add_u32 v64, 0x19d00, v91\n"
  "ds_write_b32 v80, v64 offset:0\n"
  "v_add_u32 v65, 0x19d40, v91\n"
  "ds_write_b32 v81, v65 offset:0\n"
  "v_add_u32 v66, 0x19d80, v91\n"
  "ds_write_b32 v82, v66 offset:0\n"
  "v_add_u32 v67, 0x19dc0, v91\n"
  "ds_write_b32 v83, v67 offset:0\n"
  "ds_write_b32 v77, v90 offset:1024\n"
        "s_waitcnt lgkmcnt(0)\n s_barrier"
        : "+{v[64:67]}"(inc), "={v[84:87]}"(s4), "={v88}"(mk), "={v89}"(base), "={v90}"(incl)
        : "{v[68:71]}"(sh), "{v[72:75]}"(nid), "{v76}"(lane * 16), "{v77}"(lane * 4), "{v78}"((u32)(nq0 * 4 + ((nn - nq0 + a0) << 18))), "{v79}"(qm),
          "{s[70:71]}"(vm[0]), "{s[72:73]}"(vm[1]), "{s[74:75]}"(vm[2]), "{s[76:77]}"(vm[3])
        : "v80", "v81", "v82", "v83", "v91", "memory");
#pragma unroll
    for (int i = NE; i < 69 - COLDP; i++) { piece(i); if (i == NE) { for (int j = 0; j < PFM; j++) prefetch(j); } }
#pragma unroll
    for (int j = PFM; j < PFE; j++) prefetch(j);
    const u32 item = ((mk >> 12) << 18) | ((base & 0xff) << 10) | qoff;
    st32(L_ITV + lane * 4, (item & qm) | (0xFFFC0000u & ~qm));
    st32(L_ITS + lane * 4, (item & sm) | (0xFFFC0000u & ~sm));
#pragma unroll
    for (int i = 0; i < 4; i++) st32(L_QN + qoff + 32 * i, (s4[i] & 0x3fc) << 8);
#pragma unroll
    for (int j = PFE; j < PFC; j++) prefetch(j);
    __builtin_amdgcn_s_setprio(0);
    nch = __builtin_amdgcn_readlane(incl, 63); novr = __builtin_amdgcn_readfirstlane(lds32(L_MISC + 4));
    asm volatile("s_waitcnt vmcnt(%0)" :: "i"(PFC - PFM) : "memory");
  } else {
    u32 o4[4];
    asm volatile("s_load_dword %0, %4, 0x0\n\ts_load_dword %1, %4, 0x100\n\ts_load_dword %2, %4, 0x200\n\ts_load_dword %3, %4, 0x300"
                 : "=&s"(o4[0]), "=&s"(o4[1]), "=&s"(o4[2]), "=&s"(o4[3]) : "s"(offs + w * 256 + bin));
    u32x2 key[4]; u32x4 pay[4];
    const u32x4 srdR = mksrd(recs, 0, NPROD * NBINS * RSTR, 0x00020000u);
    const u32 rbase = (w * 4 * NBINS + bin) * RSTR;
#pragma unroll
    for (int k = 0; k < 4; k++)
      asm volatile("buffer_load_dwordx2 %0, %1, %2, %3 offen" : "=v"(key[k]) : "v"(lane < 48 ? lane * 8 : 0x7ffffff0u), "s"(srdR), "s"(rbase + k * (NBINS * RSTR)));
    clear();
    asm volatile("s_waitcnt lgkmcnt(0)\n\ts_barrier" : "+s"(o4[0]), "+s"(o4[1]), "+s"(o4[2]), "+s"(o4[3]) :: "memory");
    if (__builtin_expect(max(max(o4[0] & 0x7fff, o4[1] & 0x7fff), max(o4[2] & 0x7fff, o4[3] & 0x7fff)) > 96, 0)) {
      asm volatile("s_waitcnt vmcnt(0)" : "+v"(key[0]), "+v"(key[1]), "+v"(key[2]), "+v"(key[3]));
#pragma unroll
      for (int k = 0; k < 4; k++)
        asm volatile("buffer_load_dwordx2 %0, %1, %2, %3 offen" : "+v"(key[k]) : "v"(lane * 2 < (o4[k] & 0x7fff) ? lane * 8 : 0x7ffffff0u), "s"(srdR), "s"(rbase + k * (NBINS * RSTR)));
    }
    unsigned long long m[8];
#pragma unroll
    for (int k = 0; k < 4; k++) {
      const u32 n = o4[k] & 0x7fff;
      m[2 * k] = __builtin_amdgcn_ballot_w64(lane * 2 < n); m[2 * k + 1] = __builtin_amdgcn_ballot_w64(lane * 2 + 1 < n);
      if (k < NPB) asm volatile("buffer_load_dwordx4 %0, %1, %2, %3 offen offset:512" : "=v"(pay[k]) : "v"(lane * 2 < n ? lane * 16 : 0x7ffffff0u), "s"(srdR), "s"(rbase + k * (NBINS * RSTR)));
    }
    u32 rank[8], na[8];
#pragma unroll
    for (int k = 0; k < 4; k++) {
      asm volatile("s_waitcnt vmcnt(%1)" : "+v"(key[k]) : "i"(3 - k + NPB));
#pragma unroll
      for (int h = 0; h < 2; h++) {
        na[2 * k + h] = L_CNT + (key[k][h] & 0xff) * 4;
        asm volatile("s_mov_b64 exec, %2\n\tds_add_rtn_u32 %0, %1, %3\n\ts_mov_b64 exec, -1" : "=v"(rank[2 * k + h]) : "v"(na[2 * k + h]), "s"(m[2 * k + h]), "v"(4u) : "memory");
      }
    }
    asm volatile("s_waitcnt lgkmcnt(0)" : "+v"(rank[0]), "+v"(rank[1]), "+v"(rank[2]), "+v"(rank[3]), "+v"(rank[4]), "+v"(rank[5]), "+v"(rank[6]), "+v"(rank[7]) :: "memory");
    if (__builtin_expect((o4[0] | o4[1] | o4[2] | o4[3]) > 0xffffu, 0)) {
#pragma unroll
      for (int k = 0; k < NPB; k++) asm volatile("s_waitcnt vmcnt(0)" : "+v"(pay[k]));
#pragma unroll
      for (int k = 0; k < 4; k++)
        for (u32 i = lane; i < (o4[k] >> 16); i += 64) {
          u32x4 r = ovf[(size_t)(w * 4 + k) * PCAP + i];
          if (r[3] == bin) {
            u32 rk = ldsadd(L_CNT + (r[2] & 0xff) * 4, 4u);
            u32 j = ldsadd(L_MISC + 4, 1u);
            if (j < MAXOVL) *(LAS u32x4*)(L_OVL + j * 16) = u32x4{r[0], r[1], r[2], rk};
          }
        }
      __builtin_amdgcn_s_waitcnt(0x0F70);
    }
    BAR();
#pragma unroll
    for (int k = NPB; k < 4; k++)
      asm volatile("buffer_load_dwordx4 %0, %1, %2, %3 offen offset:512" : "=v"(pay[k]) : "v"(lane * 2 < (o4[k] & 0x7fff) ? lane * 16 : 0x7ffffff0u), "s"(srdR), "s"(rbase + k * (NBINS * RSTR)));
#pragma unroll
    for (int k = 0; k < 8; k++) { rank[k] = rankoff(rank[k]); key[k >> 1][k & 1] = min(key[k >> 1][k & 1] & ~0xffu, smax) + 768u; asm volatile("" : "+v"(rank[k]), "+v"(key[k >> 1])); }
    BAR();
    u32 ni[8];
#pragma unroll
    for (int k = 0; k < 8; k++) asm volatile("s_mov_b64 exec, %2\n\tds_read_b32 %0, %1\n\ts_mov_b64 exec, -1" : "=v"(ni[k]) : "v"(na[k]), "s"(m[k]) : "memory");
    u32 m0 = lds32(L_HIST + 252), m1 = lds32(L_MISC + 4);
    asm volatile("s_waitcnt lgkmcnt(0)" : "+v"(ni[0]), "+v"(ni[1]), "+v"(ni[2]), "+v"(ni[3]), "+v"(ni[4]), "+v"(ni[5]), "+v"(ni[6]), "+v"(ni[7]), "+v"(m0), "+v"(m1));
    nch = __builtin_amdgcn_readfirstlane(m0); novr = __builtin_amdgcn_readfirstlane(m1);
    if (__builtin_expect((nch <= MAXCH) && (novr <= MAXOVL), 1)) {
#pragma unroll
      for (int k = 0; k < 8; k++) {
        u32 t0, t1;
        asm volatile("s_mov_b64 exec, %5\n\tv_and_b32 %2, 0xc0, %0\n\tv_add_u32 %0, %0, %3\n\tv_mad_i32_i24 %2, %2, -4, %4\n\t"
                     "v_lshrrev_b32 %1, 1, %0\n\tds_write_b32 %1, %2 offset:%6\n\ts_mov_b64 exec, -1"
                     : "+v"(ni[k]), "=&v"(t0), "=&v"(t1) : "v"(rank[k]), "v"(key[k >> 1][k & 1]), "s"(m[k]), "i"(L_SRC - L_REC / 2) : "memory");
      }
#pragma unroll
      for (int k = 0; k < 4; k++) {
        asm volatile("s_waitcnt vmcnt(%1)" : "+v"(pay[k]) : "i"(3 - k));
        u32x2 p0 = {pay[k][0], pay[k][1]}, p1 = {pay[k][2], pay[k][3]};
        asm volatile("s_mov_b64 exec, %4\n\tds_write_b64 %0, %1\n\ts_mov_b64 exec, %5\n\tds_write_b64 %2, %3\n\ts_mov_b64 exec, -1"
                     :: "v"(ni[2 * k]), "v"(p0), "v"(ni[2 * k + 1]), "v"(p1), "s"(m[2 * k]), "s"(m[2 * k + 1]) : "memory");
      }
      if (__builtin_expect(novr != 0, 0) && t < novr) { u32x4 r = lds128(L_OVL + t * 16); place(r[0], r[1], min(r[2] & ~0xffu, smax), lds32(L_CNT + (r[2] & 0xff) * 4) + rankoff(r[3])); }
    }
    if (__builtin_expect((o4[0] | o4[1] | o4[2] | o4[3]) & 0x8000u, 0)) {
#pragma unroll 1
      for (int i = 1; i <= COLDP; i++) piece(-i);
    }
    asm volatile("s_waitcnt vmcnt(0)" : "+v"(pay[0]), "+v"(pay[1]), "+v"(pay[2]), "+v"(pay[3]) :: "memory");
  }
  const bool fast = (nch <= MAXCH) && (novr <= MAXOVL);
  if (__builtin_expect(fast, 1)) {
    const u32x4 srdS = mksrd((const char*)sv + (size_t)n0 * 1024, 0, nn * 1024, 0x00020000u);
    const u32x4 srdO = mksrd((char*)out + (size_t)n0 * 1024, 0, nn * 1024, 0x00020000u);
    phase_sca(mksrd(tab + (size_t)4 * N * 256 - 768, 16, 0xffffffffu, 0x00800000u), srdS, srdO, w);
    phase_vec(mksrd(tab + (size_t)u * N * 256 - 768, 16, 0xffffffffu, 0x00800000u),
              mksrd((const char*)sv + (size_t)n0 * 1024 + 64 * u, 0, nn * 1024 - (nn ? 64 * u : 0), 0x00020000u),
              mksrd((char*)out + (size_t)n0 * 1024 + 64 * u, 0, nn * 1024 - (nn ? 64 * u : 0), 0x00020000u));
  } else {
    BAR();
    const int gl = lane_id(), p = gl & 15, tt = w * 64 + gl, grp = tt >> 4, q0 = u * QN;
#pragma unroll 1
    for (int pass = 0; pass < 2; pass++) {
      const int nacc = pass ? QN * 128 : BIN * 96;
      for (int i = tt; i < nacc; i += 1024) st32(L_SRC + i * 4, 0);
      BAR();
      const char* tb = tab + (size_t)(pass ? 4 : u) * N * 256 + p * 16;
#pragma unroll 1
      for (int pr = 0; pr < NPROD; pr++) {
        const u32 o = offs[pr * 64 + bin], n = o & 0x7fff, tot = n + (o >> 16);
#pragma unroll 1
        for (u32 i = grp; i < tot; i += 64) {
          u32 r0, r1, r2; bool use = true;
          if (i < n) { const u32* r = (const u32*)(recs + (size_t)(pr * NBINS + bin) * RSTR); r0 = r[128 + 2 * i]; r1 = r[129 + 2 * i]; r2 = r[i]; }
          else { u32x4 r = ovf[(size_t)pr * PCAP + (i - n)]; r0 = r[0]; r1 = r[1]; r2 = r[2]; use = r[3] == (u32)bin; }
          const int dl = (r2 & 0xff) - (pass ? q0 : 0);
          if (pass && (u32)dl >= (u32)QN) use = false;
          if (use) {
            const u32x4 x = *(const u32x4*)(tb + min(r2 & ~0xffu, smax));
            const u32 lr = L_LUT + ((r1 >> 16) - LUT_BASE_BITS) * 32;
            float val[8] = {__uint_as_float(x[0]), __uint_as_float(x[1]), __uint_as_float(x[0] << 16), __uint_as_float((x[1] << 16) | (x[0] >> 16)),
                            __uint_as_float(x[2] << 16), __uint_as_float((x[3] << 16) | (x[2] >> 16)), __uint_as_float(x[2]), __uint_as_float(x[3])};
#pragma unroll
            for (int j = 0; j < 8; j++) {
              const u32 wb = (pass ? L_WS + (j >> 1) * 1024 : L_WV + (j >= 6 ? 1024 : 0)) + (p + 32 * (j & 1)) * 16;
              float sum = 0.f;
#pragma unroll 1
              for (int k = 0; k < 16; k++) sum = fmaf(b2f(*(LAS u16*)(lr + k * 2)), b2f(*(LAS u16*)(wb + (k >> 3) * 256 + (k & 7) * 2)), sum);
              val[j] *= sum;
            }
            if (pass) {
#pragma unroll
              for (int j = 0; j < 8; j++) __hip_atomic_fetch_add((LAS float*)(L_SRC + (dl * 128 + 8 * p + j) * 4), val[j], __ATOMIC_RELAXED, __HIP_MEMORY_SCOPE_WORKGROUP);
            } else {
              const float rh[3] = {b2f(r0), h2f(r0), b2f(r1)};
#pragma unroll
              for (int j = 0; j < 6; j++)
                __hip_atomic_fetch_add((LAS float*)(L_SRC + (dl * 96 + (j >> 1) * 32 + 2 * p + (j & 1)) * 4), fmaf(val[6 + (j & 1)], rh[j >> 1], val[j]), __ATOMIC_RELAXED, __HIP_MEMORY_SCOPE_WORKGROUP);
            }
          }
        }
      }
      BAR();
      for (int i = tt; i < nacc; i += 1024) {
        const int nd = pass ? q0 + (i >> 7) : i / 96, c = pass ? (i & 127) : 128 + ((i % 96) >> 5) * 128 + 32 * u + (i & 31);
        if (nd < nn) { const size_t e = (size_t)(n0 + nd) * 512 + c; out[e] = (u16)(pk2(b2f(sv[e]) + __uint_as_float(lds32(L_SRC + i * 4)), 0.f) & 0xffff); }
      }
      BAR();
    }
  }
}

extern "C" __global__ void g_phi(const u16* sv, const u16* w1, const u16* w2, float* phi, float* acc) {
  __shared__ float s[128], h[128];
  const int n = blockIdx.x, t = threadIdx.x;
  if (t < 128) s[t] = b2f(sv[(size_t)n * 512 + t]);
  for (int i = t; i < 512; i += 384) acc[(size_t)n * 512 + i] = 0.f;
  __syncthreads();
  if (t < 128) { float a = 0; for (int k = 0; k < 128; k++) a += s[k] * b2f(w1[k * 128 + t]); a = rbh(a); h[t] = rbh(a / (1.f + __expf(-a))); }
  __syncthreads();
  float a = 0;
  for (int k = 0; k < 128; k++) a += h[k] * b2f(w2[k * 384 + t]);
  phi[(size_t)n * 384 + t] = a;
}
__global__ void g_edge(const u16* sv, const u16* pos, const int* src, const int* dst, const u16* freqs, const u16* wr, const float* phi, float* acc) {
  const int e = blockIdx.x, t = threadIdx.x;
  const size_t s = src[e], d_ = dst[e];
  const float rx = rbh(b2f(pos[s * 3]) - b2f(pos[d_ * 3])), ry = rbh(b2f(pos[s * 3 + 1]) - b2f(pos[d_ * 3 + 1])), rz = rbh(b2f(pos[s * 3 + 2]) - b2f(pos[d_ * 3 + 2]));
  const float d = rbh(sqrtf(rbh(rbh(rbh(rx * rx) + rbh(ry * ry) + rbh(rz * rz)) + 1e-8f)));
  if (!(d < 5.0f)) return;
  const float fc = rbh(0.5f * rbh(rbh(cosf(rbh(rbh(3.141592653589793f * d) / 5.0f))) + 1.0f));
  float ws = 0, wv = 0, wx = 0;
  for (int k = 0; k < 16; k++) { float a = rbh(rbh(sinf(rbh(d * b2f(freqs[k])))) / d); ws += a * b2f(wr[k * 384 + t]); wv += a * b2f(wr[k * 384 + 128 + t]); wx += a * b2f(wr[k * 384 + 256 + t]); }
  const float mv = phi[s * 384 + 128 + t] * wv * fc, mx = phi[s * 384 + 256 + t] * wx * fc;
  atomicAdd(&acc[d_ * 512 + t], phi[s * 384 + t] * ws * fc);
  atomicAdd(&acc[d_ * 512 + 128 + t], mv * b2f(sv[s * 512 + 128 + t]) + mx * rbh(rx / d));
  atomicAdd(&acc[d_ * 512 + 256 + t], mv * b2f(sv[s * 512 + 256 + t]) + mx * rbh(ry / d));
  atomicAdd(&acc[d_ * 512 + 384 + t], mv * b2f(sv[s * 512 + 384 + t]) + mx * rbh(rz / d));
}
__global__ void g_out(const u16* sv, const float* acc, u16* out) {
  const size_t i = (size_t)blockIdx.x * 512 + threadIdx.x;
  out[i] = (u16)(pk2(b2f(sv[i]) + acc[i], 0.f) & 0xffff);
}

extern "C" void kernel_launch(void* const* d_in, const int* in_sizes, int n_in, void* d_out, int out_size, void* d_ws, size_t ws_size, hipStream_t stream) {
  const u16* sv = (const u16*)d_in[0]; const u16* pos = (const u16*)d_in[1]; const int* src = (const int*)d_in[2]; const int* dst = (const int*)d_in[3];
  const u16* fr = (const u16*)d_in[4]; const u16* w1 = (const u16*)d_in[5]; const u16* w2 = (const u16*)d_in[6]; const u16* wr = (const u16*)d_in[7];
  int N = in_sizes[0] / 512, E = in_sizes[2];
  char* ws = (char*)d_ws;
  char* lut = ws;
  char* tab = ws + LUT_BYTES;
  char* wfrag = tab + (size_t)5 * N * 256;
  int EP = (((E + NPROD - 1) / NPROD) + 3) & ~3;
  const bool tuned = N >= 4 && N <= BIN * NBINS && (N & 1) == 0 && EP <= PCAP && (size_t)(wfrag - ws) + W_END <= ws_size;
  if (!tuned) {
    float* phi = (float*)d_ws; float* acc = phi + (size_t)N * 384;
    g_phi<<<N, 384, 0, stream>>>(sv, w1, w2, phi, acc);
    if (E > 0) g_edge<<<E, 128, 0, stream>>>(sv, pos, src, dst, fr, wr, phi, acc);
    g_out<<<N, 512, 0, stream>>>(sv, acc, (u16*)d_out);
    return;
  }
  (void)hipFuncSetAttribute((const void*)k_main, hipFuncAttributeMaxDynamicSharedMemorySize, 163840);
  (void)hipFuncSetAttribute((const void*)k_prep, hipFuncAttributeMaxDynamicSharedMemorySize, 163840);
  k_prep<<<256, 1024, 163840, stream>>>(sv, pos, src, dst, fr, w1, w2, wr, ws, N, E, EP);
  k_main<<<256, 1024, 163840, stream>>>(sv, (u16*)d_out, wfrag, lut, N);
}
```
